```python
import torch

def _softmax(x, axis=-1):
    return torch.softmax(x, dim=axis)
def _gelu(x):
    return torch.nn.functional.gelu(x, approximate="tanh")
def _silu(x):
    return torch.nn.functional.silu(x)
def _sigmoid(x):
    return torch.sigmoid(x)
def _layernorm(x, axis=-1, eps=1e-5):
    m = x.mean(dim=axis, keepdim=True)
    v = x.var(dim=axis, keepdim=True, unbiased=False)
    return (x - m) / torch.sqrt(v + eps)
def _scatter_add(tgt, idx, src, axis=0):
    return tgt.clone().index_add_(axis, idx, src)
def _segment_sum(src, seg, n):
    out = torch.zeros((n,) + src.shape[1:], dtype=src.dtype)
    return out.index_add_(0, seg, src)
def _pad(x, pw):
    flat = tuple(v for pair in reversed(pw) for v in pair)
    return torch.nn.functional.pad(x, flat)

kernel_name = 'gated_mix__gqa_block_cached__gqa_block_cached__decode'

def setup_inputs():

    _w = (lambda a: (lambda t: t.to(torch.bfloat16) if t.is_floating_point() else t)(torch.as_tensor(a)))
    def _A_setup():
        B, Sq, D = 2, 1, 448
        Hq, Hkv, Sk, Dh = 16, 8, 8192, 256
        assert Hq % Hkv == 0, (Hq, Hkv)
        return {
            "x": _w((lambda *s: torch.randn(*s, dtype=torch.bfloat16) * 0.05)(B, Sq, D)),
            "k_cache": _w((lambda *s: torch.randn(*s, dtype=torch.bfloat16) * 6.4)(B, Hkv, Sk, Dh)),
            "v_cache": _w((lambda *s: torch.randn(*s, dtype=torch.bfloat16) * 0.05)(B, Hkv, Sk, Dh)),
            "wq": _w((lambda *s: torch.randn(*s, dtype=torch.bfloat16) * 6.4)(D, Hq * Dh) * (D ** -0.5)),
            "wo": _w((lambda *s: torch.randn(*s, dtype=torch.bfloat16) * 0.05)(Hq * Dh, D) * ((Hq * Dh) ** -0.5)),
        }
    _A_in = _A_setup()

    def _B_setup():
        B, Sq, D = 2, 1, 448
        Hq, Hkv, Sk, Dh = 16, 8, 8192, 256
        assert Hq % Hkv == 0, (Hq, Hkv)
        return {
            "x": _w((lambda *s: torch.randn(*s, dtype=torch.bfloat16) * 0.05)(B, Sq, D)),
            "k_cache": _w((lambda *s: torch.randn(*s, dtype=torch.bfloat16) * 6.4)(B, Hkv, Sk, Dh)),
            "v_cache": _w((lambda *s: torch.randn(*s, dtype=torch.bfloat16) * 0.05)(B, Hkv, Sk, Dh)),
            "wq": _w((lambda *s: torch.randn(*s, dtype=torch.bfloat16) * 6.4)(D, Hq * Dh) * (D ** -0.5)),
            "wo": _w((lambda *s: torch.randn(*s, dtype=torch.bfloat16) * 0.05)(Hq * Dh, D) * ((Hq * Dh) ** -0.5)),
        }
    _B_in = _B_setup()

    return {
        "A_x": _A_in["x"],
        "A_k_cache": _A_in["k_cache"],
        "A_v_cache": _A_in["v_cache"],
        "A_wq": _A_in["wq"],
        "A_wo": _A_in["wo"],
        "B_x": _B_in["x"],
        "B_k_cache": _B_in["k_cache"],
        "B_v_cache": _B_in["v_cache"],
        "B_wq": _B_in["wq"],
        "B_wo": _B_in["wo"],
        "gate_w": _w((lambda *s: torch.randn(*s, dtype=torch.bfloat16) * 0.05)(448) * 16.0),
    }

def reference(A_x, A_k_cache, A_v_cache, A_wq, A_wo, B_x, B_k_cache, B_v_cache, B_wq, B_wo, gate_w):
    def _A_ref(x, k_cache, v_cache, wq, wo):
        B, Sq, D = x.shape
        Hq, Dh = 16, 256
        Hkv = k_cache.shape[1]
        rep = Hq // Hkv
        q = (lambda x, *d: x.permute(*d))((x @ wq).reshape(B, Sq, Hq, Dh), 0, 2, 1, 3)
        k = (lambda x, n, axis: torch.repeat_interleave(x, n, dim=axis))(k_cache, rep, axis=1)
        v = (lambda x, n, axis: torch.repeat_interleave(x, n, dim=axis))(v_cache, rep, axis=1)
        scores = torch.einsum("bhqd,bhkd->bhqk", q, k) * (Dh ** -0.5)
        p = _softmax(scores, axis=-1)
        o = torch.einsum("bhqk,bhkd->bhqd", p, v)
        return (lambda x, *d: x.permute(*d))(o, 0, 2, 1, 3).reshape(B, Sq, Hq * Dh) @ wo

    def _B_ref(x, k_cache, v_cache, wq, wo):
        B, Sq, D = x.shape
        Hq, Dh = 16, 256
        Hkv = k_cache.shape[1]
        rep = Hq // Hkv
        q = (lambda x, *d: x.permute(*d))((x @ wq).reshape(B, Sq, Hq, Dh), 0, 2, 1, 3)
        k = (lambda x, n, axis: torch.repeat_interleave(x, n, dim=axis))(k_cache, rep, axis=1)
        v = (lambda x, n, axis: torch.repeat_interleave(x, n, dim=axis))(v_cache, rep, axis=1)
        scores = torch.einsum("bhqd,bhkd->bhqk", q, k) * (Dh ** -0.5)
        p = _softmax(scores, axis=-1)
        o = torch.einsum("bhqk,bhkd->bhqd", p, v)
        return (lambda x, *d: x.permute(*d))(o, 0, 2, 1, 3).reshape(B, Sq, Hq * Dh) @ wo

    x = A_x
    g = _sigmoid((x * gate_w).sum(-1, keepdims=True))
    a = _A_ref(x, A_k_cache, A_v_cache, A_wq, A_wo)
    b = _B_ref(x, B_k_cache, B_v_cache, B_wq, B_wo)
    return g * a + (1 - g) * b
```

```cpp
#include <hip/hip_runtime.h>
#include <hip/hip_bf16.h>
#include <stdint.h>

typedef unsigned v4u __attribute__((ext_vector_type(4)));
typedef unsigned v2u __attribute__((ext_vector_type(2)));
typedef float f32x2 __attribute__((ext_vector_type(2)));
typedef float f32x4 __attribute__((ext_vector_type(4)));
typedef __bf16 bf16x2 __attribute__((ext_vector_type(2)));
#define GAS __attribute__((address_space(1)))

#define STAMPS 0

constexpr int DM = 448, NQ = 4096;
constexpr int NWG = 256, NTHR = 512;
constexpr int WIN = 32;
constexpr int DEPTH = 4;
constexpr int ROT_KB = 136;
constexpr unsigned SPIN_CAP = 1u << 16;

constexpr int WS_EPOCH = 0;
constexpr int WS_QG = 4096;
constexpr int P1_STRIDE = 4096 + 128;
constexpr int WS_P1 = WS_QG + 32 * 2048;
constexpr int P2_STRIDE = 896 * 8;
constexpr int WS_P2 = WS_P1 + NWG * P1_STRIDE;
constexpr int WS_END = WS_P2 + NWG * P2_STRIDE;
constexpr int WS_STAMP = (WS_END + 4095) & ~4095;

constexpr int L_P = 0;
constexpr int L_ACC = L_P + 8 * 256;
constexpr int L_ML = L_ACC + 8 * 512;
constexpr int L_QP = L_ML + 32;
constexpr int L_O = L_QP + 7 * 64;
constexpr int L_RED = L_O + 64;
constexpr int L_G = L_RED + 32;
constexpr int L_TOTAL = L_G + 4;

struct Params {
  const uint16_t* x;
  const uint16_t* k[2];
  const uint16_t* v[2];
  const uint16_t* wq[2];
  const uint16_t* wo[2];
  const uint16_t* gw;
  uint16_t* out;
  unsigned char* ws;
};

__device__ __forceinline__ float u2f(unsigned u) { return __builtin_bit_cast(float, u); }
__device__ __forceinline__ unsigned f2u(float f) { return __builtin_bit_cast(unsigned, f); }
__device__ __forceinline__ float dot2(unsigned k, unsigned q, float acc) {
  return __builtin_amdgcn_fdot2_f32_bf16(__builtin_bit_cast(bf16x2, k), __builtin_bit_cast(bf16x2, q), acc, false);
}
__device__ __forceinline__ float bf_lo(unsigned u) { return u2f(u << 16); }
__device__ __forceinline__ float bf_hi(unsigned u) { return u2f(u & 0xffff0000u); }
__device__ __forceinline__ unsigned short f2bf(float f) { return __builtin_bit_cast(unsigned short, (__bf16)f); }
__device__ __forceinline__ float rbf(float f) { return (float)(__bf16)f; }
template <int CTRL> __device__ __forceinline__ float dpp(float x) {
  return u2f(__builtin_amdgcn_mov_dpp(__builtin_bit_cast(int, x), CTRL, 0xf, 0xf, true));
}
constexpr int XOR1 = 0xB1, XOR2 = 0x4E, XOR7 = 0x141, ROR8 = 0x128, ROR4 = 0x124;
__device__ __forceinline__ float tr16(float a, float b) {
  auto r = __builtin_amdgcn_permlane16_swap(f2u(a), f2u(b), false, false);
  return u2f(r[0]) + u2f(r[1]);
}
__device__ __forceinline__ float tr32(float a, float b) {
  auto r = __builtin_amdgcn_permlane32_swap(f2u(a), f2u(b), false, false);
  return u2f(r[0]) + u2f(r[1]);
}
template <int CTRL> __device__ __forceinline__ float trd(float a, float b, bool bit) {
  const float t1 = a + dpp<CTRL>(a), t2 = b + dpp<CTRL>(b);
  return bit ? t2 : t1;
}
__device__ __forceinline__ float max16(float x) {
  auto r = __builtin_amdgcn_permlane16_swap(f2u(x), f2u(x), false, false);
  return fmaxf(u2f(r[0]), u2f(r[1]));
}
__device__ __forceinline__ float max32(float x) {
  auto r = __builtin_amdgcn_permlane32_swap(f2u(x), f2u(x), false, false);
  return fmaxf(u2f(r[0]), u2f(r[1]));
}
__device__ __forceinline__ v4u ldnt(const GAS unsigned char* p) { return __builtin_nontemporal_load((const GAS v4u*)p); }
#define SB() __builtin_amdgcn_sched_barrier(0)
#define PACE() asm volatile("s_waitcnt vmcnt(%0)" ::"i"(DEPTH - 1) : "memory")
constexpr int AUX_SC1 = 16;

__device__ __forceinline__ float reduce_batch(const float (&x)[16][2], int lane) {
  float y[8][2], z[4][2], u[2][2];
#pragma unroll
  for (int i = 0; i < 8; ++i) { y[i][0] = tr16(x[i][0], x[i + 8][0]); y[i][1] = tr16(x[i][1], x[i + 8][1]); }
#pragma unroll
  for (int i = 0; i < 4; ++i) { z[i][0] = trd<ROR8>(y[i][0], y[i + 4][0], lane & 8); z[i][1] = trd<ROR8>(y[i][1], y[i + 4][1], lane & 8); }
#pragma unroll
  for (int i = 0; i < 2; ++i) { u[i][0] = trd<XOR7>(z[i][0], z[i + 2][0], lane & 4); u[i][1] = trd<XOR7>(z[i][1], z[i + 2][1], lane & 4); }
  const float w0 = trd<XOR2>(u[0][0], u[1][0], lane & 2), w1 = trd<XOR2>(u[0][1], u[1][1], lane & 2);
  return trd<XOR1>(w0, w1, lane & 1);
}
#define DOTS(X, KV) do { float d0_ = 0.f, d1_ = 0.f; \
    _Pragma("unroll") for (int c_ = 0; c_ < 4; ++c_) { d0_ = dot2((KV)[c_], q0[c_], d0_); d1_ = dot2((KV)[c_], q1[c_], d1_); } \
    (X)[0] = d0_; (X)[1] = d1_; } while (0)

#define STAMP(i) do {} while (0)

__global__ void __launch_bounds__(NTHR, 2) gqa2_fused(Params p) {
  __shared__ __attribute__((aligned(16))) float lds[L_TOTAL];
  const int wg = blockIdx.x, tid = threadIdx.x, lane = tid & 63, wave = __builtin_amdgcn_readfirstlane(tid >> 6);
  const int g = wg >> 3, s = wg & 7, br = g >> 4, b = (g >> 3) & 1, kvh = g & 7, j = 2 * s + b;
  const __amdgpu_buffer_rsrc_t rs = __builtin_amdgcn_make_buffer_rsrc((void*)p.ws, 0, WS_END, 0x00020000);
  const unsigned lane16 = lane * 16u;

  const unsigned ep = __builtin_amdgcn_raw_buffer_load_b32(rs, WS_EPOCH, 0, AUX_SC1);
  v4u wqv[4];
  v2u xv[2];
  const int rid = 28 * s + g;
  if (wave < 7) {
    const int row0 = 64 * wave + 4 * (lane >> 2);
    const GAS unsigned char* wp = (const GAS unsigned char*)p.wq[br] + (size_t)(kvh * 512 + 32 * j) * 2;
#pragma unroll
    for (int i = 0; i < 4; ++i) wqv[i] = ldnt(wp + (unsigned)((row0 + i) * (NQ * 2) + (lane & 3) * 16));
    xv[0] = *(const GAS v2u*)((const GAS unsigned char*)p.x + row0 * 2);
    xv[1] = *(const GAS v2u*)((const GAS unsigned char*)p.x + (DM + row0) * 2);
  } else {
    const int l = lane < 56 ? lane : 0, bo = (4 * rid >= DM) ? 1 : 0;
    wqv[0] = ldnt((const GAS unsigned char*)p.x + bo * (DM * 2) + l * 16);
    wqv[1] = ldnt((const GAS unsigned char*)p.gw + l * 16);
    wqv[2] = wqv[3] = v4u{0u, 0u, 0u, 0u};
    xv[0] = xv[1] = v2u{0u, 0u};
  }
  const GAS unsigned char* kb = (const GAS unsigned char*)p.k[br] + (size_t)(g & 15) * (4u << 20);
  const GAS unsigned char* vb = (const GAS unsigned char*)p.v[br] + (size_t)(g & 15) * (4u << 20);
  const unsigned kb0 = (unsigned)((s * 8 + wave) * 2 + g * ROT_KB);
#define SOFF(n) ((((kb0 + ((n) >> 1) * 128u) & 4095u) + ((n) & 1)) << 10)
  v4u R[WIN];
#pragma unroll
  for (int i = 0; i < DEPTH; ++i) R[i] = ldnt(kb + SOFF(i) + lane16);
  SB();

  if (wave < 7) {
    float a[2][8];
#pragma unroll
    for (int k = 0; k < 8; ++k) a[0][k] = a[1][k] = 0.f;
#pragma unroll
    for (int i = 0; i < 4; ++i) {
      const float x0 = (i & 1) ? bf_hi(xv[0][i >> 1]) : bf_lo(xv[0][i >> 1]);
      const float x1 = (i & 1) ? bf_hi(xv[1][i >> 1]) : bf_lo(xv[1][i >> 1]);
#pragma unroll
      for (int c = 0; c < 4; ++c) {
        const float lo = bf_lo(wqv[i][c]), hi = bf_hi(wqv[i][c]);
        a[0][2 * c] = fmaf(x0, lo, a[0][2 * c]); a[0][2 * c + 1] = fmaf(x0, hi, a[0][2 * c + 1]);
        a[1][2 * c] = fmaf(x1, lo, a[1][2 * c]); a[1][2 * c + 1] = fmaf(x1, hi, a[1][2 * c + 1]);
      }
    }
    float y[8], z[4], u[2];
#pragma unroll
    for (int k = 0; k < 8; ++k) y[k] = tr32(a[0][k], a[1][k]);
#pragma unroll
    for (int k = 0; k < 4; ++k) z[k] = tr16(y[k], y[k + 4]);
#pragma unroll
    for (int k = 0; k < 2; ++k) u[k] = trd<ROR8>(z[k], z[k + 2], lane & 8);
    const float keep = (lane & 4) ? u[1] : u[0], send = (lane & 4) ? u[0] : u[1];
    const float o4a = dpp<0x124>(send), o4b = dpp<0x12C>(send);
    const float o4 = (lane & 4) ? o4a : o4b;
    const float r = keep + o4;
    lds[L_QP + wave * 64 + (lane >> 5) * 32 + (lane & 3) * 8 + ((lane >> 2) & 7)] = r;
  } else {
    float z = 0.f;
#pragma unroll
    for (int c = 0; c < 4; ++c) z += rbf(bf_lo(wqv[0][c]) * bf_lo(wqv[1][c])) + rbf(bf_hi(wqv[0][c]) * bf_hi(wqv[1][c]));
    if (lane >= 56) z = 0.f;
    z += dpp<XOR1>(z); z += dpp<XOR2>(z); z += dpp<ROR4>(z); z += dpp<ROR8>(z);
    z = tr16(z, z); z = tr32(z, z);
    z = rbf(z);
    if (lane == 0) lds[L_G] = rbf(1.f / (1.f + __expf(-z)));
  }
  const unsigned tag = __builtin_amdgcn_readfirstlane(ep) + 1u;
  __syncthreads();
  if (wave == 0 && lane < 16) {
    f32x4 q4 = *(const f32x4*)&lds[L_QP + 4 * lane];
#pragma unroll
    for (int w = 1; w < 7; ++w) q4 += *(const f32x4*)&lds[L_QP + w * 64 + 4 * lane];
    const v4u gr = {(unsigned)f2bf(q4[0]) | ((unsigned)f2bf(q4[1]) << 16), tag, (unsigned)f2bf(q4[2]) | ((unsigned)f2bf(q4[3]) << 16), tag};
    __builtin_amdgcn_raw_buffer_store_b128(gr, rs, WS_QG + ((br * 8 + kvh) * 2 + (lane >> 3)) * 2048 + j * 128 + (lane & 7) * 16, 0, AUX_SC1);
  }
  STAMP(1);

  const int qoff = WS_QG + ((br * 8 + kvh) * 2 + b) * 2048 + lane * 32;
  v4u qa, qb;
#pragma unroll
  for (int i = DEPTH; i < WIN; ++i) {
    PACE();
    R[i] = ldnt(kb + SOFF(i) + lane16);
    if (i == WIN - 6) {
      qa = __builtin_amdgcn_raw_buffer_load_b128(rs, qoff, 0, AUX_SC1);
      qb = __builtin_amdgcn_raw_buffer_load_b128(rs, qoff + 16, 0, AUX_SC1);
    }
    SB();
  }
  STAMP(2);
  if (__builtin_expect(!__all(qa[1] == tag && qa[3] == tag && qb[1] == tag && qb[3] == tag), 0)) {
    for (unsigned spins = 0; spins < SPIN_CAP; ++spins) {
      asm volatile("" ::: "memory");
      qa = __builtin_amdgcn_raw_buffer_load_b128(rs, qoff, 0, AUX_SC1);
      qb = __builtin_amdgcn_raw_buffer_load_b128(rs, qoff + 16, 0, AUX_SC1);
      if (__all(qa[1] == tag && qa[3] == tag && qb[1] == tag && qb[3] == tag)) break;
      __builtin_amdgcn_s_sleep(1);
    }
  }
  unsigned q0[4], q1[4];
  {
    const unsigned qq[4] = {qa[0], qa[2], qb[0], qb[2]};
#pragma unroll
    for (int c = 0; c < 4; ++c) { auto r = __builtin_amdgcn_permlane32_swap(qq[c], qq[c], false, false); q0[c] = r[0]; q1[c] = r[1]; }
  }
  STAMP(3);

  v4u S[DEPTH];
  float sc[4];
#pragma unroll
  for (int bt = 0; bt < WIN / 16; ++bt) {
    float x[16][2];
#pragma unroll
    for (int i = 0; i < 16; ++i) {
      const int n = bt * 16 + i;
      if (n % (WIN / DEPTH) == 0) { S[n / (WIN / DEPTH)] = ldnt(kb + SOFF(WIN + n / (WIN / DEPTH)) + lane16); SB(); }
      DOTS(x[i], R[n]);
    }
    sc[bt] = reduce_batch(x, lane);
  }
  STAMP(4);
  sc[2] = 0.f;
#pragma unroll 1
  for (int bt = WIN / 16; bt < 4; ++bt) {
    float x[16][2];
#pragma unroll
    for (int i = 0; i < 16; ++i) {
      DOTS(x[i], S[i % DEPTH]);
      const int n = bt * 16 + i + DEPTH;
      const GAS unsigned char* base = (i + DEPTH >= 16 && bt == 3) ? vb : kb;
      S[i % DEPTH] = ldnt(base + SOFF(n & 63) + lane16);
      SB();
    }
    const float v = reduce_batch(x, lane);
    sc[2] = (bt == 2) ? v : sc[2];
    sc[3] = v;
  }
  STAMP(5);
  {
    constexpr float C2 = 0.0625f * 1.4426950408889634f;
    float m = fmaxf(fmaxf(sc[0], sc[1]), fmaxf(sc[2], sc[3]));
    m = fmaxf(m, dpp<XOR2>(m)); m = fmaxf(m, dpp<ROR4>(m)); m = fmaxf(m, dpp<ROR8>(m)); m = max16(m); m = max32(m);
    m *= C2;
    float l = 0.f;
#pragma unroll
    for (int i = 0; i < 4; ++i) { const float e = __builtin_amdgcn_exp2f(fmaf(sc[i], C2, -m)); l += e; lds[L_P + wave * 256 + i * 64 + lane] = e; }
    l += dpp<XOR2>(l); l += dpp<ROR4>(l); l += dpp<ROR8>(l); l = tr16(l, l); l = tr32(l, l);
    if (lane < 2) *(f32x2*)&lds[L_ML + wave * 4 + lane * 2] = f32x2{m, l};
  }
  f32x2 acc[2][4];
#pragma unroll
  for (int c = 0; c < 4; ++c) acc[0][c] = acc[1][c] = f32x2{0.f, 0.f};
  const float* pl = &lds[L_P + wave * 256 + (lane >> 5) * 32];
#define PV(VV, P0, P1) do { _Pragma("unroll") for (int c_ = 0; c_ < 4; ++c_) { const f32x2 vf_ = {bf_lo((VV)[c_]), bf_hi((VV)[c_])}; \
    acc[0][c_] = __builtin_elementwise_fma(f32x2{(P0), (P0)}, vf_, acc[0][c_]); acc[1][c_] = __builtin_elementwise_fma(f32x2{(P1), (P1)}, vf_, acc[1][c_]); } } while (0)
  static_assert(DEPTH == 4, "V loop is written for a ring of 4");
#pragma unroll 1
  for (int it = 0; it < 15; ++it) {
    const f32x4 pa = *(const f32x4*)(pl + (it >> 2) * 64 + (it & 3) * 8), pb = *(const f32x4*)(pl + (it >> 2) * 64 + (it & 3) * 8 + 4);
    PV(S[0], pa[0], pa[1]); S[0] = ldnt(vb + SOFF(it * 4 + 4) + lane16); SB();
    PV(S[1], pa[2], pa[3]); S[1] = ldnt(vb + SOFF(it * 4 + 5) + lane16); SB();
    PV(S[2], pb[0], pb[1]); S[2] = ldnt(vb + SOFF(it * 4 + 6) + lane16); SB();
    PV(S[3], pb[2], pb[3]); S[3] = ldnt(vb + SOFF(it * 4 + 7) + lane16); SB();
  }
  v4u wov[4];
  {
    const int wv = wave < 7 ? wave : 0;
    const GAS unsigned char* wp = (const GAS unsigned char*)p.wo[br] + (size_t)(kvh * 512 + 32 * j) * (DM * 2) + wv * 128;
#pragma unroll
    for (int i = 0; i < 4; ++i) wov[i] = ldnt(wp + (unsigned)((4 * (lane >> 3) + i) * (DM * 2) + (lane & 7) * 16));
    SB();
  }
  {
    const f32x4 pa = *(const f32x4*)(pl + 3 * 64 + 3 * 8), pb = *(const f32x4*)(pl + 3 * 64 + 3 * 8 + 4);
    PV(S[0], pa[0], pa[1]); PV(S[1], pa[2], pa[3]); PV(S[2], pb[0], pb[1]); PV(S[3], pb[2], pb[3]);
  }
  STAMP(6);
  {
    f32x4 o0, o1;
    o0[0] = tr32(acc[0][0][0], acc[1][0][0]); o0[1] = tr32(acc[0][0][1], acc[1][0][1]);
    o0[2] = tr32(acc[0][1][0], acc[1][1][0]); o0[3] = tr32(acc[0][1][1], acc[1][1][1]);
    o1[0] = tr32(acc[0][2][0], acc[1][2][0]); o1[1] = tr32(acc[0][2][1], acc[1][2][1]);
    o1[2] = tr32(acc[0][3][0], acc[1][3][0]); o1[3] = tr32(acc[0][3][1], acc[1][3][1]);
    *(f32x4*)&lds[L_ACC + wave * 512 + lane * 8] = o0;
    *(f32x4*)&lds[L_ACC + wave * 512 + lane * 8 + 4] = o1;
  }
  __syncthreads();
  if (tid < 256) {
    const int h = tid >> 7;
    float mw[8], M = -INFINITY;
#pragma unroll
    for (int w = 0; w < 8; ++w) { mw[w] = lds[L_ML + w * 4 + h * 2]; M = fmaxf(M, mw[w]); }
    float L = 0.f; f32x2 o = {0.f, 0.f};
#pragma unroll
    for (int w = 0; w < 8; ++w) {
      const float f = __builtin_amdgcn_exp2f(mw[w] - M);
      L = fmaf(f, lds[L_ML + w * 4 + h * 2 + 1], L);
      const f32x2 a = *(const f32x2*)&lds[L_ACC + w * 512 + 2 * tid];
      o[0] = fmaf(f, a[0], o[0]); o[1] = fmaf(f, a[1], o[1]);
    }
    const v4u gr = {f2u(o[0]), tag, f2u(o[1]), tag};
    __builtin_amdgcn_raw_buffer_store_b128(gr, rs, WS_P1 + wg * P1_STRIDE + tid * 16, 0, AUX_SC1);
    if ((tid & 127) == 0) {
      const v4u st = {f2u(M), tag, f2u(L), tag};
      __builtin_amdgcn_raw_buffer_store_b128(st, rs, WS_P1 + wg * P1_STRIDE + 4096 + h * 16, 0, AUX_SC1);
    }
  }
  STAMP(7);
  if (wave >= 6) {
    const int bb = wave - 6, sp = lane & 7, rg = lane >> 3;
    const int src = WS_P1 + (((br * 2 + bb) * 8 + kvh) * 8 + sp) * P1_STRIDE;
    const int doff = src + (32 * j + 4 * rg) * 8, soff = src + 4096 + (j >> 3) * 16;
    v4u ga, gb, gs;
    for (unsigned spins = 0;; ++spins) {
      asm volatile("" ::: "memory");
      ga = __builtin_amdgcn_raw_buffer_load_b128(rs, doff, 0, AUX_SC1);
      gb = __builtin_amdgcn_raw_buffer_load_b128(rs, doff + 16, 0, AUX_SC1);
      gs = __builtin_amdgcn_raw_buffer_load_b128(rs, soff, 0, AUX_SC1);
      if (__all(ga[1] == tag && ga[3] == tag && gb[1] == tag && gb[3] == tag && gs[1] == tag && gs[3] == tag) || spins >= SPIN_CAP) break;
      __builtin_amdgcn_s_sleep(1);
    }
    const float ms = u2f(gs[0]), ls = u2f(gs[2]);
    float M = fmaxf(ms, dpp<XOR1>(ms)); M = fmaxf(M, dpp<XOR2>(M)); M = fmaxf(M, dpp<XOR7>(M));
    const float f = __builtin_amdgcn_exp2f(ms - M);
    float L = f * ls; L += dpp<XOR1>(L); L += dpp<XOR2>(L); L += dpp<XOR7>(L);
    float o[4] = {f * u2f(ga[0]), f * u2f(ga[2]), f * u2f(gb[0]), f * u2f(gb[2])};
    const float inv = 1.f / L;
#pragma unroll
    for (int i = 0; i < 4; ++i) { o[i] += dpp<XOR1>(o[i]); o[i] += dpp<XOR2>(o[i]); o[i] += dpp<XOR7>(o[i]); o[i] *= inv; }
    if (sp == 0) *(f32x4*)&lds[L_O + bb * 32 + 4 * rg] = f32x4{o[0], o[1], o[2], o[3]};
  }
  __syncthreads();
  STAMP(8);
  if (wave < 7) {
    const f32x4 oa = *(const f32x4*)&lds[L_O + 4 * (lane >> 3)], ob = *(const f32x4*)&lds[L_O + 32 + 4 * (lane >> 3)];
    float a[2][8];
#pragma unroll
    for (int k = 0; k < 8; ++k) a[0][k] = a[1][k] = 0.f;
#pragma unroll
    for (int i = 0; i < 4; ++i)
#pragma unroll
      for (int c = 0; c < 4; ++c) {
        const float lo = bf_lo(wov[i][c]), hi = bf_hi(wov[i][c]);
        a[0][2 * c] = fmaf(oa[i], lo, a[0][2 * c]); a[0][2 * c + 1] = fmaf(oa[i], hi, a[0][2 * c + 1]);
        a[1][2 * c] = fmaf(ob[i], lo, a[1][2 * c]); a[1][2 * c + 1] = fmaf(ob[i], hi, a[1][2 * c + 1]);
      }
    float y[8], z[4];
#pragma unroll
    for (int k = 0; k < 8; ++k) y[k] = tr32(a[0][k], a[1][k]);
#pragma unroll
    for (int k = 0; k < 4; ++k) z[k] = tr16(y[k], y[k + 4]);
    const float u0 = trd<ROR8>(z[0], z[2], lane & 8), u1 = trd<ROR8>(z[1], z[3], lane & 8);
    const int oi = (lane >> 5) * DM + 64 * wave + 8 * (lane & 7) + ((lane >> 4) & 1) * 4 + ((lane >> 3) & 1) * 2;
    const v4u gr = {f2u(u0), tag, f2u(u1), tag};
    __builtin_amdgcn_raw_buffer_store_b128(gr, rs, WS_P2 + wg * P2_STRIDE + oi * 8, 0, AUX_SC1);
  }
  STAMP(9);
  if (g < 28) {
    const int off = WS_P2 + (tid >> 1) * P2_STRIDE + rid * 32 + (tid & 1) * 16;
    v4u gy;
    for (unsigned spins = 0;; ++spins) {
      asm volatile("" ::: "memory");
      gy = __builtin_amdgcn_raw_buffer_load_b128(rs, off, 0, AUX_SC1);
      if (__all(gy[1] == tag && gy[3] == tag) || spins >= SPIN_CAP) break;
      __builtin_amdgcn_s_sleep(1);
    }
    float y = trd<XOR2>(u2f(gy[0]), u2f(gy[2]), lane & 2);
    y += dpp<ROR4>(y); y += dpp<ROR8>(y); y = tr16(y, y); y = tr32(y, y);
    if (lane < 4) lds[L_RED + wave * 4 + (lane & 1) * 2 + (lane >> 1)] = y;
    __syncthreads();
    if (tid == 0) {
      f32x4 a = *(const f32x4*)&lds[L_RED], bsum = *(const f32x4*)&lds[L_RED + 16];
#pragma unroll
      for (int w = 1; w < 4; ++w) { a += *(const f32x4*)&lds[L_RED + 4 * w]; bsum += *(const f32x4*)&lds[L_RED + 16 + 4 * w]; }
      const float gt = lds[L_G], og = rbf(1.f - gt);
      unsigned short r[4];
#pragma unroll
      for (int i = 0; i < 4; ++i) r[i] = f2bf(rbf(gt * rbf(a[i])) + rbf(og * rbf(bsum[i])));
      *(GAS v2u*)((GAS unsigned char*)p.out + rid * 8) = v2u{(unsigned)r[0] | ((unsigned)r[1] << 16), (unsigned)r[2] | ((unsigned)r[3] << 16)};
      if (wg == 0) __builtin_amdgcn_raw_buffer_store_b32(tag, rs, WS_EPOCH, 0, AUX_SC1);
    }
  }
  STAMP(10);
}

extern "C" void kernel_launch(void* const* d_in, const int* in_sizes, int n_in, void* d_out, int out_size, void* d_ws, size_t ws_size,
                              hipStream_t stream) {
  Params p;
  p.x = (const uint16_t*)d_in[0];
  p.k[0] = (const uint16_t*)d_in[1]; p.v[0] = (const uint16_t*)d_in[2]; p.wq[0] = (const uint16_t*)d_in[3]; p.wo[0] = (const uint16_t*)d_in[4];
  p.k[1] = (const uint16_t*)d_in[6]; p.v[1] = (const uint16_t*)d_in[7]; p.wq[1] = (const uint16_t*)d_in[8]; p.wo[1] = (const uint16_t*)d_in[9];
  p.gw = (const uint16_t*)d_in[10];
  p.out = (uint16_t*)d_out;
  p.ws = (unsigned char*)d_ws;
  hipLaunchKernelGGL(gqa2_fused, dim3(NWG), dim3(NTHR), 0, stream, p);
}
```

```cpp
#include <hip/hip_runtime.h>
#include <hip/hip_bf16.h>
#include <stdint.h>
#include <stdio.h>

typedef unsigned v4u __attribute__((ext_vector_type(4)));
typedef unsigned v2u __attribute__((ext_vector_type(2)));
typedef float f32x2 __attribute__((ext_vector_type(2)));
typedef float f32x4 __attribute__((ext_vector_type(4)));
typedef __bf16 bf16x2 __attribute__((ext_vector_type(2)));
#define GAS __attribute__((address_space(1)))

#define STAMPS 0

constexpr int DM = 448, NQ = 4096;
constexpr int NWG = 256, NTHR = 512;
constexpr int WIN = 32;
constexpr int DEPTH = 4;
constexpr int ROT_KB = 136;
constexpr unsigned SPIN_CAP = 1u << 16;

constexpr int WS_EPOCH = 0;
constexpr int WS_QG = 4096;
constexpr int P1_STRIDE = 4096 + 128;
constexpr int WS_P1 = WS_QG + 32 * 2048;
constexpr int P2_STRIDE = 896 * 8;
constexpr int WS_P2 = WS_P1 + NWG * P1_STRIDE;
constexpr int WS_END = WS_P2 + NWG * P2_STRIDE;
constexpr int WS_STAMP = (WS_END + 4095) & ~4095;

constexpr int L_P = 0;
constexpr int L_ACC = L_P + 8 * 256;
constexpr int L_ML = L_ACC + 8 * 512;
constexpr int L_QP = L_ML + 32;
constexpr int L_O = L_QP + 7 * 64;
constexpr int L_RED = L_O + 64;
constexpr int L_G = L_RED + 32;
constexpr int L_TOTAL = L_G + 4;

struct Params {
  const uint16_t* x;
  const uint16_t* k[2];
  const uint16_t* v[2];
  const uint16_t* wq[2];
  const uint16_t* wo[2];
  const uint16_t* gw;
  uint16_t* out;
  unsigned char* ws;
};

__device__ __forceinline__ float u2f(unsigned u) { return __builtin_bit_cast(float, u); }
__device__ __forceinline__ unsigned f2u(float f) { return __builtin_bit_cast(unsigned, f); }
__device__ __forceinline__ float dot2(unsigned k, unsigned q, float acc) {
  return __builtin_amdgcn_fdot2_f32_bf16(__builtin_bit_cast(bf16x2, k), __builtin_bit_cast(bf16x2, q), acc, false);
}
__device__ __forceinline__ float bf_lo(unsigned u) { return u2f(u << 16); }
__device__ __forceinline__ float bf_hi(unsigned u) { return u2f(u & 0xffff0000u); }
__device__ __forceinline__ unsigned short f2bf(float f) { return __builtin_bit_cast(unsigned short, (__bf16)f); }
__device__ __forceinline__ float rbf(float f) { return (float)(__bf16)f; }
template <int CTRL> __device__ __forceinline__ float dpp(float x) {
  return u2f(__builtin_amdgcn_mov_dpp(__builtin_bit_cast(int, x), CTRL, 0xf, 0xf, true));
}
constexpr int XOR1 = 0xB1, XOR2 = 0x4E, XOR7 = 0x141, ROR8 = 0x128, ROR4 = 0x124;
__device__ __forceinline__ float tr16(float a, float b) {
  auto r = __builtin_amdgcn_permlane16_swap(f2u(a), f2u(b), false, false);
  return u2f(r[0]) + u2f(r[1]);
}
__device__ __forceinline__ float tr32(float a, float b) {
  auto r = __builtin_amdgcn_permlane32_swap(f2u(a), f2u(b), false, false);
  return u2f(r[0]) + u2f(r[1]);
}
template <int CTRL> __device__ __forceinline__ float trd(float a, float b, bool bit) {
  const float t1 = a + dpp<CTRL>(a), t2 = b + dpp<CTRL>(b);
  return bit ? t2 : t1;
}
__device__ __forceinline__ float max16(float x) {
  auto r = __builtin_amdgcn_permlane16_swap(f2u(x), f2u(x), false, false);
  return fmaxf(u2f(r[0]), u2f(r[1]));
}
__device__ __forceinline__ float max32(float x) {
  auto r = __builtin_amdgcn_permlane32_swap(f2u(x), f2u(x), false, false);
  return fmaxf(u2f(r[0]), u2f(r[1]));
}
__device__ __forceinline__ v4u ldnt(const GAS unsigned char* p) { return __builtin_nontemporal_load((const GAS v4u*)p); }
#define SB() __builtin_amdgcn_sched_barrier(0)
#define PACE() asm volatile("s_waitcnt vmcnt(%0)" ::"i"(DEPTH - 1) : "memory")
constexpr int AUX_SC1 = 16;

__device__ __forceinline__ float reduce_batch(const float (&x)[16][2], int lane) {
  float y[8][2], z[4][2], u[2][2];
#pragma unroll
  for (int i = 0; i < 8; ++i) { y[i][0] = tr16(x[i][0], x[i + 8][0]); y[i][1] = tr16(x[i][1], x[i + 8][1]); }
#pragma unroll
  for (int i = 0; i < 4; ++i) { z[i][0] = trd<ROR8>(y[i][0], y[i + 4][0], lane & 8); z[i][1] = trd<ROR8>(y[i][1], y[i + 4][1], lane & 8); }
#pragma unroll
  for (int i = 0; i < 2; ++i) { u[i][0] = trd<XOR7>(z[i][0], z[i + 2][0], lane & 4); u[i][1] = trd<XOR7>(z[i][1], z[i + 2][1], lane & 4); }
  const float w0 = trd<XOR2>(u[0][0], u[1][0], lane & 2), w1 = trd<XOR2>(u[0][1], u[1][1], lane & 2);
  return trd<XOR1>(w0, w1, lane & 1);
}
#define DOTS(X, KV) do { float d0_ = 0.f, d1_ = 0.f; \
    _Pragma("unroll") for (int c_ = 0; c_ < 4; ++c_) { d0_ = dot2((KV)[c_], q0[c_], d0_); d1_ = dot2((KV)[c_], q1[c_], d1_); } \
    (X)[0] = d0_; (X)[1] = d1_; } while (0)

#define STAMP(i) do {} while (0)

__global__ void __launch_bounds__(NTHR, 2) gqa2_fused(Params p) {
  __shared__ __attribute__((aligned(16))) float lds[L_TOTAL];
  const int wg = blockIdx.x, tid = threadIdx.x, lane = tid & 63, wave = __builtin_amdgcn_readfirstlane(tid >> 6);
  const int g = wg >> 3, s = wg & 7, br = g >> 4, b = (g >> 3) & 1, kvh = g & 7, j = 2 * s + b;
  const __amdgpu_buffer_rsrc_t rs = __builtin_amdgcn_make_buffer_rsrc((void*)p.ws, 0, WS_END, 0x00020000);
  const unsigned lane16 = lane * 16u;

  const unsigned ep = __builtin_amdgcn_raw_buffer_load_b32(rs, WS_EPOCH, 0, AUX_SC1);
  v4u wqv[4];
  v2u xv[2];
  const int rid = 28 * s + g;
  if (wave < 7) {
    const int row0 = 64 * wave + 4 * (lane >> 2);
    const GAS unsigned char* wp = (const GAS unsigned char*)p.wq[br] + (size_t)(kvh * 512 + 32 * j) * 2;
#pragma unroll
    for (int i = 0; i < 4; ++i) wqv[i] = ldnt(wp + (unsigned)((row0 + i) * (NQ * 2) + (lane & 3) * 16));
    xv[0] = *(const GAS v2u*)((const GAS unsigned char*)p.x + row0 * 2);
    xv[1] = *(const GAS v2u*)((const GAS unsigned char*)p.x + (DM + row0) * 2);
  } else {
    const int l = lane < 56 ? lane : 0, bo = (4 * rid >= DM) ? 1 : 0;
    wqv[0] = ldnt((const GAS unsigned char*)p.x + bo * (DM * 2) + l * 16);
    wqv[1] = ldnt((const GAS unsigned char*)p.gw + l * 16);
    wqv[2] = wqv[3] = v4u{0u, 0u, 0u, 0u};
    xv[0] = xv[1] = v2u{0u, 0u};
  }
  const GAS unsigned char* kb = (const GAS unsigned char*)p.k[br] + (size_t)(g & 15) * (4u << 20);
  const GAS unsigned char* vb = (const GAS unsigned char*)p.v[br] + (size_t)(g & 15) * (4u << 20);
  const unsigned kb0 = (unsigned)((s * 8 + wave) * 2 + g * ROT_KB);
#define SOFF(n) ((((kb0 + ((n) >> 1) * 128u) & 4095u) + ((n) & 1)) << 10)
  v4u R[WIN];
#pragma unroll
  for (int i = 0; i < DEPTH; ++i) R[i] = ldnt(kb + SOFF(i) + lane16);
  SB();

  if (wave < 7) {
    float a[2][8];
#pragma unroll
    for (int k = 0; k < 8; ++k) a[0][k] = a[1][k] = 0.f;
#pragma unroll
    for (int i = 0; i < 4; ++i) {
      const float x0 = (i & 1) ? bf_hi(xv[0][i >> 1]) : bf_lo(xv[0][i >> 1]);
      const float x1 = (i & 1) ? bf_hi(xv[1][i >> 1]) : bf_lo(xv[1][i >> 1]);
#pragma unroll
      for (int c = 0; c < 4; ++c) {
        const float lo = bf_lo(wqv[i][c]), hi = bf_hi(wqv[i][c]);
        a[0][2 * c] = fmaf(x0, lo, a[0][2 * c]); a[0][2 * c + 1] = fmaf(x0, hi, a[0][2 * c + 1]);
        a[1][2 * c] = fmaf(x1, lo, a[1][2 * c]); a[1][2 * c + 1] = fmaf(x1, hi, a[1][2 * c + 1]);
      }
    }
    float y[8], z[4], u[2];
#pragma unroll
    for (int k = 0; k < 8; ++k) y[k] = tr32(a[0][k], a[1][k]);
#pragma unroll
    for (int k = 0; k < 4; ++k) z[k] = tr16(y[k], y[k + 4]);
#pragma unroll
    for (int k = 0; k < 2; ++k) u[k] = trd<ROR8>(z[k], z[k + 2], lane & 8);
    const float keep = (lane & 4) ? u[1] : u[0], send = (lane & 4) ? u[0] : u[1];
    const float o4a = dpp<0x124>(send), o4b = dpp<0x12C>(send);
    const float o4 = (lane & 4) ? o4a : o4b;
    const float r = keep + o4;
    lds[L_QP + wave * 64 + (lane >> 5) * 32 + (lane & 3) * 8 + ((lane >> 2) & 7)] = r;
  } else {
    float z = 0.f;
#pragma unroll
    for (int c = 0; c < 4; ++c) z += rbf(bf_lo(wqv[0][c]) * bf_lo(wqv[1][c])) + rbf(bf_hi(wqv[0][c]) * bf_hi(wqv[1][c]));
    if (lane >= 56) z = 0.f;
    z += dpp<XOR1>(z); z += dpp<XOR2>(z); z += dpp<ROR4>(z); z += dpp<ROR8>(z);
    z = tr16(z, z); z = tr32(z, z);
    z = rbf(z);
    if (lane == 0) lds[L_G] = rbf(1.f / (1.f + __expf(-z)));
  }
  const unsigned tag = __builtin_amdgcn_readfirstlane(ep) + 1u;
  __syncthreads();
  if (wave == 0 && lane < 16) {
    f32x4 q4 = *(const f32x4*)&lds[L_QP + 4 * lane];
#pragma unroll
    for (int w = 1; w < 7; ++w) q4 += *(const f32x4*)&lds[L_QP + w * 64 + 4 * lane];
    const v4u gr = {(unsigned)f2bf(q4[0]) | ((unsigned)f2bf(q4[1]) << 16), tag, (unsigned)f2bf(q4[2]) | ((unsigned)f2bf(q4[3]) << 16), tag};
    __builtin_amdgcn_raw_buffer_store_b128(gr, rs, WS_QG + ((br * 8 + kvh) * 2 + (lane >> 3)) * 2048 + j * 128 + (lane & 7) * 16, 0, AUX_SC1);
  }
  STAMP(1);

  const int qoff = WS_QG + ((br * 8 + kvh) * 2 + b) * 2048 + lane * 32;
  v4u qa, qb;
#pragma unroll
  for (int i = DEPTH; i < WIN; ++i) {
    PACE();
    R[i] = ldnt(kb + SOFF(i) + lane16);
    if (i == WIN - 6) {
      qa = __builtin_amdgcn_raw_buffer_load_b128(rs, qoff, 0, AUX_SC1);
      qb = __builtin_amdgcn_raw_buffer_load_b128(rs, qoff + 16, 0, AUX_SC1);
    }
    SB();
  }
  STAMP(2);
  if (__builtin_expect(!__all(qa[1] == tag && qa[3] == tag && qb[1] == tag && qb[3] == tag), 0)) {
    for (unsigned spins = 0; spins < SPIN_CAP; ++spins) {
      asm volatile("" ::: "memory");
      qa = __builtin_amdgcn_raw_buffer_load_b128(rs, qoff, 0, AUX_SC1);
      qb = __builtin_amdgcn_raw_buffer_load_b128(rs, qoff + 16, 0, AUX_SC1);
      if (__all(qa[1] == tag && qa[3] == tag && qb[1] == tag && qb[3] == tag)) break;
      __builtin_amdgcn_s_sleep(1);
    }
  }
  unsigned q0[4], q1[4];
  {
    const unsigned qq[4] = {qa[0], qa[2], qb[0], qb[2]};
#pragma unroll
    for (int c = 0; c < 4; ++c) { auto r = __builtin_amdgcn_permlane32_swap(qq[c], qq[c], false, false); q0[c] = r[0]; q1[c] = r[1]; }
  }
  STAMP(3);

  v4u S[DEPTH];
  float sc[4];
#pragma unroll
  for (int bt = 0; bt < WIN / 16; ++bt) {
    float x[16][2];
#pragma unroll
    for (int i = 0; i < 16; ++i) {
      const int n = bt * 16 + i;
      if (n % (WIN / DEPTH) == 0) { S[n / (WIN / DEPTH)] = ldnt(kb + SOFF(WIN + n / (WIN / DEPTH)) + lane16); SB(); }
      DOTS(x[i], R[n]);
    }
    sc[bt] = reduce_batch(x, lane);
  }
  STAMP(4);
  sc[2] = 0.f;
#pragma unroll 1
  for (int bt = WIN / 16; bt < 4; ++bt) {
    float x[16][2];
#pragma unroll
    for (int i = 0; i < 16; ++i) {
      DOTS(x[i], S[i % DEPTH]);
      const int n = bt * 16 + i + DEPTH;
      const GAS unsigned char* base = (i + DEPTH >= 16 && bt == 3) ? vb : kb;
      S[i % DEPTH] = ldnt(base + SOFF(n & 63) + lane16);
      SB();
    }
    const float v = reduce_batch(x, lane);
    sc[2] = (bt == 2) ? v : sc[2];
    sc[3] = v;
  }
  STAMP(5);
  {
    constexpr float C2 = 0.0625f * 1.4426950408889634f;
#pragma unroll
    for (int i = 0; i < 4; ++i) sc[i] = rbf(sc[i]);
    float m = fmaxf(fmaxf(sc[0], sc[1]), fmaxf(sc[2], sc[3]));
    m = fmaxf(m, dpp<XOR2>(m)); m = fmaxf(m, dpp<ROR4>(m)); m = fmaxf(m, dpp<ROR8>(m)); m = max16(m); m = max32(m);
    m *= C2;
    float l = 0.f;
#pragma unroll
    for (int i = 0; i < 4; ++i) { const float e = __builtin_amdgcn_exp2f(fmaf(sc[i], C2, -m)); l += e; lds[L_P + wave * 256 + i * 64 + lane] = e; }
    l += dpp<XOR2>(l); l += dpp<ROR4>(l); l += dpp<ROR8>(l); l = tr16(l, l); l = tr32(l, l);
    if (lane < 2) *(f32x2*)&lds[L_ML + wave * 4 + lane * 2] = f32x2{m, l};
  }
  f32x2 acc[2][4];
#pragma unroll
  for (int c = 0; c < 4; ++c) acc[0][c] = acc[1][c] = f32x2{0.f, 0.f};
  const float* pl = &lds[L_P + wave * 256 + (lane >> 5) * 32];
#define PV(VV, P0, P1) do { _Pragma("unroll") for (int c_ = 0; c_ < 4; ++c_) { const f32x2 vf_ = {bf_lo((VV)[c_]), bf_hi((VV)[c_])}; \
    acc[0][c_] = __builtin_elementwise_fma(f32x2{(P0), (P0)}, vf_, acc[0][c_]); acc[1][c_] = __builtin_elementwise_fma(f32x2{(P1), (P1)}, vf_, acc[1][c_]); } } while (0)
  static_assert(DEPTH == 4, "V loop is written for a ring of 4");
#pragma unroll 1
  for (int it = 0; it < 15; ++it) {
    const f32x4 pa = *(const f32x4*)(pl + (it >> 2) * 64 + (it & 3) * 8), pb = *(const f32x4*)(pl + (it >> 2) * 64 + (it & 3) * 8 + 4);
    PV(S[0], pa[0], pa[1]); S[0] = ldnt(vb + SOFF(it * 4 + 4) + lane16); SB();
    PV(S[1], pa[2], pa[3]); S[1] = ldnt(vb + SOFF(it * 4 + 5) + lane16); SB();
    PV(S[2], pb[0], pb[1]); S[2] = ldnt(vb + SOFF(it * 4 + 6) + lane16); SB();
    PV(S[3], pb[2], pb[3]); S[3] = ldnt(vb + SOFF(it * 4 + 7) + lane16); SB();
  }
  v4u wov[4];
  {
    const int wv = wave < 7 ? wave : 0;
    const GAS unsigned char* wp = (const GAS unsigned char*)p.wo[br] + (size_t)(kvh * 512 + 32 * j) * (DM * 2) + wv * 128;
#pragma unroll
    for (int i = 0; i < 4; ++i) wov[i] = ldnt(wp + (unsigned)((4 * (lane >> 3) + i) * (DM * 2) + (lane & 7) * 16));
    SB();
  }
  {
    const f32x4 pa = *(const f32x4*)(pl + 3 * 64 + 3 * 8), pb = *(const f32x4*)(pl + 3 * 64 + 3 * 8 + 4);
    PV(S[0], pa[0], pa[1]); PV(S[1], pa[2], pa[3]); PV(S[2], pb[0], pb[1]); PV(S[3], pb[2], pb[3]);
  }
  STAMP(6);
  {
    f32x4 o0, o1;
    o0[0] = tr32(acc[0][0][0], acc[1][0][0]); o0[1] = tr32(acc[0][0][1], acc[1][0][1]);
    o0[2] = tr32(acc[0][1][0], acc[1][1][0]); o0[3] = tr32(acc[0][1][1], acc[1][1][1]);
    o1[0] = tr32(acc[0][2][0], acc[1][2][0]); o1[1] = tr32(acc[0][2][1], acc[1][2][1]);
    o1[2] = tr32(acc[0][3][0], acc[1][3][0]); o1[3] = tr32(acc[0][3][1], acc[1][3][1]);
    *(f32x4*)&lds[L_ACC + wave * 512 + lane * 8] = o0;
    *(f32x4*)&lds[L_ACC + wave * 512 + lane * 8 + 4] = o1;
  }
  __syncthreads();
  if (tid < 256) {
    const int h = tid >> 7;
    float mw[8], M = -INFINITY;
#pragma unroll
    for (int w = 0; w < 8; ++w) { mw[w] = lds[L_ML + w * 4 + h * 2]; M = fmaxf(M, mw[w]); }
    float L = 0.f; f32x2 o = {0.f, 0.f};
#pragma unroll
    for (int w = 0; w < 8; ++w) {
      const float f = __builtin_amdgcn_exp2f(mw[w] - M);
      L = fmaf(f, lds[L_ML + w * 4 + h * 2 + 1], L);
      const f32x2 a = *(const f32x2*)&lds[L_ACC + w * 512 + 2 * tid];
      o[0] = fmaf(f, a[0], o[0]); o[1] = fmaf(f, a[1], o[1]);
    }
    const v4u gr = {f2u(o[0]), tag, f2u(o[1]), tag};
    __builtin_amdgcn_raw_buffer_store_b128(gr, rs, WS_P1 + wg * P1_STRIDE + tid * 16, 0, AUX_SC1);
    if ((tid & 127) == 0) {
      const v4u st = {f2u(M), tag, f2u(L), tag};
      __builtin_amdgcn_raw_buffer_store_b128(st, rs, WS_P1 + wg * P1_STRIDE + 4096 + h * 16, 0, AUX_SC1);
    }
  }
  STAMP(7);
  if (wave >= 6) {
    const int bb = wave - 6, sp = lane & 7, rg = lane >> 3;
    const int src = WS_P1 + (((br * 2 + bb) * 8 + kvh) * 8 + sp) * P1_STRIDE;
    const int doff = src + (32 * j + 4 * rg) * 8, soff = src + 4096 + (j >> 3) * 16;
    v4u ga, gb, gs;
    for (unsigned spins = 0;; ++spins) {
      asm volatile("" ::: "memory");
      ga = __builtin_amdgcn_raw_buffer_load_b128(rs, doff, 0, AUX_SC1);
      gb = __builtin_amdgcn_raw_buffer_load_b128(rs, doff + 16, 0, AUX_SC1);
      gs = __builtin_amdgcn_raw_buffer_load_b128(rs, soff, 0, AUX_SC1);
      if (__all(ga[1] == tag && ga[3] == tag && gb[1] == tag && gb[3] == tag && gs[1] == tag && gs[3] == tag) || spins >= SPIN_CAP) break;
      __builtin_amdgcn_s_sleep(1);
    }
    const float ms = u2f(gs[0]), ls = u2f(gs[2]);
    float M = fmaxf(ms, dpp<XOR1>(ms)); M = fmaxf(M, dpp<XOR2>(M)); M = fmaxf(M, dpp<XOR7>(M));
    const float f = __builtin_amdgcn_exp2f(ms - M);
    float L = f * ls; L += dpp<XOR1>(L); L += dpp<XOR2>(L); L += dpp<XOR7>(L);
    float o[4] = {f * u2f(ga[0]), f * u2f(ga[2]), f * u2f(gb[0]), f * u2f(gb[2])};
    const float inv = 1.f / L;
#pragma unroll
    for (int i = 0; i < 4; ++i) { o[i] += dpp<XOR1>(o[i]); o[i] += dpp<XOR2>(o[i]); o[i] += dpp<XOR7>(o[i]); o[i] = rbf(o[i] * inv); }
    if (sp == 0) *(f32x4*)&lds[L_O + bb * 32 + 4 * rg] = f32x4{o[0], o[1], o[2], o[3]};
  }
  __syncthreads();
  STAMP(8);
  if (wave < 7) {
    const f32x4 oa = *(const f32x4*)&lds[L_O + 4 * (lane >> 3)], ob = *(const f32x4*)&lds[L_O + 32 + 4 * (lane >> 3)];
    float a[2][8];
#pragma unroll
    for (int k = 0; k < 8; ++k) a[0][k] = a[1][k] = 0.f;
#pragma unroll
    for (int i = 0; i < 4; ++i)
#pragma unroll
      for (int c = 0; c < 4; ++c) {
        const float lo = bf_lo(wov[i][c]), hi = bf_hi(wov[i][c]);
        a[0][2 * c] = fmaf(oa[i], lo, a[0][2 * c]); a[0][2 * c + 1] = fmaf(oa[i], hi, a[0][2 * c + 1]);
        a[1][2 * c] = fmaf(ob[i], lo, a[1][2 * c]); a[1][2 * c + 1] = fmaf(ob[i], hi, a[1][2 * c + 1]);
      }
    float y[8], z[4];
#pragma unroll
    for (int k = 0; k < 8; ++k) y[k] = tr32(a[0][k], a[1][k]);
#pragma unroll
    for (int k = 0; k < 4; ++k) z[k] = tr16(y[k], y[k + 4]);
    const float u0 = trd<ROR8>(z[0], z[2], lane & 8), u1 = trd<ROR8>(z[1], z[3], lane & 8);
    const int oi = (lane >> 5) * DM + 64 * wave + 8 * (lane & 7) + ((lane >> 4) & 1) * 4 + ((lane >> 3) & 1) * 2;
    const v4u gr = {f2u(u0), tag, f2u(u1), tag};
    __builtin_amdgcn_raw_buffer_store_b128(gr, rs, WS_P2 + wg * P2_STRIDE + oi * 8, 0, AUX_SC1);
  }
  STAMP(9);
  if (g < 28) {
    const int off = WS_P2 + (tid >> 1) * P2_STRIDE + rid * 32 + (tid & 1) * 16;
    v4u gy;
    for (unsigned spins = 0;; ++spins) {
      asm volatile("" ::: "memory");
      gy = __builtin_amdgcn_raw_buffer_load_b128(rs, off, 0, AUX_SC1);
      if (__all(gy[1] == tag && gy[3] == tag) || spins >= SPIN_CAP) break;
      __builtin_amdgcn_s_sleep(1);
    }
    float y = trd<XOR2>(u2f(gy[0]), u2f(gy[2]), lane & 2);
    y += dpp<ROR4>(y); y += dpp<ROR8>(y); y = tr16(y, y); y = tr32(y, y);
    if (lane < 4) lds[L_RED + wave * 4 + (lane & 1) * 2 + (lane >> 1)] = y;
    __syncthreads();
    if (tid == 0) {
      f32x4 a = *(const f32x4*)&lds[L_RED], bsum = *(const f32x4*)&lds[L_RED + 16];
#pragma unroll
      for (int w = 1; w < 4; ++w) { a += *(const f32x4*)&lds[L_RED + 4 * w]; bsum += *(const f32x4*)&lds[L_RED + 16 + 4 * w]; }
      const float gt = lds[L_G], og = rbf(1.f - gt);
      unsigned short r[4];
#pragma unroll
      for (int i = 0; i < 4; ++i) r[i] = f2bf(rbf(gt * rbf(a[i])) + rbf(og * rbf(bsum[i])));
      *(GAS v2u*)((GAS unsigned char*)p.out + rid * 8) = v2u{(unsigned)r[0] | ((unsigned)r[1] << 16), (unsigned)r[2] | ((unsigned)r[3] << 16)};
      if (wg == 0) __builtin_amdgcn_raw_buffer_store_b32(tag, rs, WS_EPOCH, 0, AUX_SC1);
    }
  }
  STAMP(10);
}

extern "C" void kernel_launch(void* const* d_in, const int* in_sizes, int n_in, void* d_out, int out_size, void* d_ws, size_t ws_size,
                              hipStream_t stream) {
  Params p;
  for (int i = 0; i < n_in; ++i) fprintf(stderr, "in%d %p\n", i, d_in[i]);
  fprintf(stderr, "out %p ws %p size %zu\n", d_out, d_ws, ws_size);
  p.x = (const uint16_t*)d_in[0];
  p.k[0] = (const uint16_t*)d_in[1]; p.v[0] = (const uint16_t*)d_in[2]; p.wq[0] = (const uint16_t*)d_in[3]; p.wo[0] = (const uint16_t*)d_in[4];
  p.k[1] = (const uint16_t*)d_in[6]; p.v[1] = (const uint16_t*)d_in[7]; p.wq[1] = (const uint16_t*)d_in[8]; p.wo[1] = (const uint16_t*)d_in[9];
  p.gw = (const uint16_t*)d_in[10];
  p.out = (uint16_t*)d_out;
  p.ws = (unsigned char*)d_ws;
  hipLaunchKernelGGL(gqa2_fused, dim3(NWG), dim3(NTHR), 0, stream, p);
}
```

```cpp
#include <hip/hip_runtime.h>
#include <hip/hip_bf16.h>
#include <stdint.h>
#include <stdio.h>

typedef unsigned v4u __attribute__((ext_vector_type(4)));
typedef unsigned v2u __attribute__((ext_vector_type(2)));
typedef float f32x2 __attribute__((ext_vector_type(2)));
typedef float f32x4 __attribute__((ext_vector_type(4)));
typedef __bf16 bf16x2 __attribute__((ext_vector_type(2)));
#define GAS __attribute__((address_space(1)))

#define STAMPS 0

constexpr int DM = 448, NQ = 4096;
constexpr int NWG = 256, NTHR = 512;
constexpr int WIN = 32;
constexpr int DEPTH = 4;
constexpr int ROT_KB = 136;
constexpr unsigned SPIN_CAP = 1u << 16;

constexpr int WS_EPOCH = 0;
constexpr int WS_QG = 4096;
constexpr int P1_STRIDE = 4096 + 128;
constexpr int WS_P1 = WS_QG + 32 * 2048;
constexpr int P2_STRIDE = 896 * 8;
constexpr int WS_P2 = WS_P1 + NWG * P1_STRIDE;
constexpr int WS_END = WS_P2 + NWG * P2_STRIDE;
constexpr int WS_STAMP = (WS_END + 4095) & ~4095;

constexpr int L_P = 0;
constexpr int L_ACC = L_P + 8 * 256;
constexpr int L_ML = L_ACC + 8 * 512;
constexpr int L_QP = L_ML + 32;
constexpr int L_O = L_QP + 7 * 64;
constexpr int L_RED = L_O + 64;
constexpr int L_G = L_RED + 32;
constexpr int L_TOTAL = L_G + 4;

struct Params {
  const uint16_t* x;
  const uint16_t* k[2];
  const uint16_t* v[2];
  const uint16_t* wq[2];
  const uint16_t* wo[2];
  const uint16_t* gw;
  uint16_t* out;
  unsigned char* ws;
};

__device__ __forceinline__ float u2f(unsigned u) { return __builtin_bit_cast(float, u); }
__device__ __forceinline__ unsigned f2u(float f) { return __builtin_bit_cast(unsigned, f); }
__device__ __forceinline__ float dot2(unsigned k, unsigned q, float acc) {
  return __builtin_amdgcn_fdot2_f32_bf16(__builtin_bit_cast(bf16x2, k), __builtin_bit_cast(bf16x2, q), acc, false);
}
__device__ __forceinline__ float bf_lo(unsigned u) { return u2f(u << 16); }
__device__ __forceinline__ float bf_hi(unsigned u) { return u2f(u & 0xffff0000u); }
__device__ __forceinline__ unsigned short f2bf(float f) { return __builtin_bit_cast(unsigned short, (__bf16)f); }
__device__ __forceinline__ float rbf(float f) { return (float)(__bf16)f; }
template <int CTRL> __device__ __forceinline__ float dpp(float x) {
  return u2f(__builtin_amdgcn_mov_dpp(__builtin_bit_cast(int, x), CTRL, 0xf, 0xf, true));
}
constexpr int XOR1 = 0xB1, XOR2 = 0x4E, XOR7 = 0x141, ROR8 = 0x128, ROR4 = 0x124;
__device__ __forceinline__ float tr16(float a, float b) {
  auto r = __builtin_amdgcn_permlane16_swap(f2u(a), f2u(b), false, false);
  return u2f(r[0]) + u2f(r[1]);
}
__device__ __forceinline__ float tr32(float a, float b) {
  auto r = __builtin_amdgcn_permlane32_swap(f2u(a), f2u(b), false, false);
  return u2f(r[0]) + u2f(r[1]);
}
template <int CTRL> __device__ __forceinline__ float trd(float a, float b, bool bit) {
  const float t1 = a + dpp<CTRL>(a), t2 = b + dpp<CTRL>(b);
  return bit ? t2 : t1;
}
__device__ __forceinline__ float max16(float x) {
  auto r = __builtin_amdgcn_permlane16_swap(f2u(x), f2u(x), false, false);
  return fmaxf(u2f(r[0]), u2f(r[1]));
}
__device__ __forceinline__ float max32(float x) {
  auto r = __builtin_amdgcn_permlane32_swap(f2u(x), f2u(x), false, false);
  return fmaxf(u2f(r[0]), u2f(r[1]));
}
__device__ __forceinline__ v4u ldnt(const GAS unsigned char* p) { return __builtin_nontemporal_load((const GAS v4u*)p); }
#define SB() __builtin_amdgcn_sched_barrier(0)
#define PACE() asm volatile("s_waitcnt vmcnt(%0)" ::"i"(DEPTH - 1) : "memory")
constexpr int AUX_SC1 = 16;

__device__ __forceinline__ float reduce_batch(const float (&x)[16][2], int lane) {
  float y[8][2], z[4][2], u[2][2];
#pragma unroll
  for (int i = 0; i < 8; ++i) { y[i][0] = tr16(x[i][0], x[i + 8][0]); y[i][1] = tr16(x[i][1], x[i + 8][1]); }
#pragma unroll
  for (int i = 0; i < 4; ++i) { z[i][0] = trd<ROR8>(y[i][0], y[i + 4][0], lane & 8); z[i][1] = trd<ROR8>(y[i][1], y[i + 4][1], lane & 8); }
#pragma unroll
  for (int i = 0; i < 2; ++i) { u[i][0] = trd<XOR7>(z[i][0], z[i + 2][0], lane & 4); u[i][1] = trd<XOR7>(z[i][1], z[i + 2][1], lane & 4); }
  const float w0 = trd<XOR2>(u[0][0], u[1][0], lane & 2), w1 = trd<XOR2>(u[0][1], u[1][1], lane & 2);
  return trd<XOR1>(w0, w1, lane & 1);
}
#define DOTS(X, KV) do { float d0_ = 0.f, d1_ = 0.f; \
    _Pragma("unroll") for (int c_ = 0; c_ < 4; ++c_) { d0_ = dot2((KV)[c_], q0[c_], d0_); d1_ = dot2((KV)[c_], q1[c_], d1_); } \
    (X)[0] = d0_; (X)[1] = d1_; } while (0)

#define STAMP(i) do { if ((i) == 3) t_k = __builtin_amdgcn_s_memrealtime(); } while (0)

__global__ void __launch_bounds__(NTHR, 2) gqa2_fused(Params p) {
  __shared__ __attribute__((aligned(16))) float lds[L_TOTAL];
  const int wg = blockIdx.x, tid = threadIdx.x, lane = tid & 63, wave = __builtin_amdgcn_readfirstlane(tid >> 6);
  const int g = wg >> 3, s = wg & 7, br = g >> 4, b = (g >> 3) & 1, kvh = g & 7, j = 2 * s + b;
  const __amdgpu_buffer_rsrc_t rs = __builtin_amdgcn_make_buffer_rsrc((void*)p.ws, 0, WS_END, 0x00020000);
  const unsigned lane16 = lane * 16u;
  unsigned long long t_k = 0;

  const unsigned ep = __builtin_amdgcn_raw_buffer_load_b32(rs, WS_EPOCH, 0, AUX_SC1);
  v4u wqv[4];
  v2u xv[2];
  const int rid = 28 * s + g;
  if (wave < 7) {
    const int row0 = 64 * wave + 4 * (lane >> 2);
    const GAS unsigned char* wp = (const GAS unsigned char*)p.wq[br] + (size_t)(kvh * 512 + 32 * j) * 2;
#pragma unroll
    for (int i = 0; i < 4; ++i) wqv[i] = ldnt(wp + (unsigned)((row0 + i) * (NQ * 2) + (lane & 3) * 16));
    xv[0] = *(const GAS v2u*)((const GAS unsigned char*)p.x + row0 * 2);
    xv[1] = *(const GAS v2u*)((const GAS unsigned char*)p.x + (DM + row0) * 2);
  } else {
    const int l = lane < 56 ? lane : 0, bo = (4 * rid >= DM) ? 1 : 0;
    wqv[0] = ldnt((const GAS unsigned char*)p.x + bo * (DM * 2) + l * 16);
    wqv[1] = ldnt((const GAS unsigned char*)p.gw + l * 16);
    wqv[2] = wqv[3] = v4u{0u, 0u, 0u, 0u};
    xv[0] = xv[1] = v2u{0u, 0u};
  }
  const GAS unsigned char* kb = (const GAS unsigned char*)p.k[br] + (size_t)(g & 15) * (4u << 20);
  const GAS unsigned char* vb = (const GAS unsigned char*)p.v[br] + (size_t)(g & 15) * (4u << 20);
  const unsigned kb0 = (unsigned)((s * 8 + wave) * 2 + g * ROT_KB);
#define SOFF(n) ((((kb0 + ((n) >> 1) * 128u) & 4095u) + ((n) & 1)) << 10)
  v4u R[WIN];
#pragma unroll
  for (int i = 0; i < DEPTH; ++i) R[i] = ldnt(kb + SOFF(i) + lane16);
  SB();

  if (wave < 7) {
    float a[2][8];
#pragma unroll
    for (int k = 0; k < 8; ++k) a[0][k] = a[1][k] = 0.f;
#pragma unroll
    for (int i = 0; i < 4; ++i) {
      const float x0 = (i & 1) ? bf_hi(xv[0][i >> 1]) : bf_lo(xv[0][i >> 1]);
      const float x1 = (i & 1) ? bf_hi(xv[1][i >> 1]) : bf_lo(xv[1][i >> 1]);
#pragma unroll
      for (int c = 0; c < 4; ++c) {
        const float lo = bf_lo(wqv[i][c]), hi = bf_hi(wqv[i][c]);
        a[0][2 * c] = fmaf(x0, lo, a[0][2 * c]); a[0][2 * c + 1] = fmaf(x0, hi, a[0][2 * c + 1]);
        a[1][2 * c] = fmaf(x1, lo, a[1][2 * c]); a[1][2 * c + 1] = fmaf(x1, hi, a[1][2 * c + 1]);
      }
    }
    float y[8], z[4], u[2];
#pragma unroll
    for (int k = 0; k < 8; ++k) y[k] = tr32(a[0][k], a[1][k]);
#pragma unroll
    for (int k = 0; k < 4; ++k) z[k] = tr16(y[k], y[k + 4]);
#pragma unroll
    for (int k = 0; k < 2; ++k) u[k] = trd<ROR8>(z[k], z[k + 2], lane & 8);
    const float keep = (lane & 4) ? u[1] : u[0], send = (lane & 4) ? u[0] : u[1];
    const float o4a = dpp<0x124>(send), o4b = dpp<0x12C>(send);
    const float o4 = (lane & 4) ? o4a : o4b;
    const float r = keep + o4;
    lds[L_QP + wave * 64 + (lane >> 5) * 32 + (lane & 3) * 8 + ((lane >> 2) & 7)] = r;
  } else {
    float z = 0.f;
#pragma unroll
    for (int c = 0; c < 4; ++c) z += rbf(bf_lo(wqv[0][c]) * bf_lo(wqv[1][c])) + rbf(bf_hi(wqv[0][c]) * bf_hi(wqv[1][c]));
    if (lane >= 56) z = 0.f;
    z += dpp<XOR1>(z); z += dpp<XOR2>(z); z += dpp<ROR4>(z); z += dpp<ROR8>(z);
    z = tr16(z, z); z = tr32(z, z);
    z = rbf(z);
    if (lane == 0) lds[L_G] = rbf(1.f / (1.f + __expf(-z)));
  }
  const unsigned tag = __builtin_amdgcn_readfirstlane(ep) + 1u;
  __syncthreads();
  if (wave == 0 && lane < 16) {
    f32x4 q4 = *(const f32x4*)&lds[L_QP + 4 * lane];
#pragma unroll
    for (int w = 1; w < 7; ++w) q4 += *(const f32x4*)&lds[L_QP + w * 64 + 4 * lane];
    const v4u gr = {(unsigned)f2bf(q4[0]) | ((unsigned)f2bf(q4[1]) << 16), tag, (unsigned)f2bf(q4[2]) | ((unsigned)f2bf(q4[3]) << 16), tag};
    __builtin_amdgcn_raw_buffer_store_b128(gr, rs, WS_QG + ((br * 8 + kvh) * 2 + (lane >> 3)) * 2048 + j * 128 + (lane & 7) * 16, 0, AUX_SC1);
  }
  STAMP(1);

  const int qoff = WS_QG + ((br * 8 + kvh) * 2 + b) * 2048 + lane * 32;
  v4u qa, qb;
#pragma unroll
  for (int i = DEPTH; i < WIN; ++i) {
    PACE();
    R[i] = ldnt(kb + SOFF(i) + lane16);
    if (i == WIN - 6) {
      qa = __builtin_amdgcn_raw_buffer_load_b128(rs, qoff, 0, AUX_SC1);
      qb = __builtin_amdgcn_raw_buffer_load_b128(rs, qoff + 16, 0, AUX_SC1);
    }
    SB();
  }
  STAMP(2);
  if (__builtin_expect(!__all(qa[1] == tag && qa[3] == tag && qb[1] == tag && qb[3] == tag), 0)) {
    for (unsigned spins = 0; spins < SPIN_CAP; ++spins) {
      asm volatile("" ::: "memory");
      qa = __builtin_amdgcn_raw_buffer_load_b128(rs, qoff, 0, AUX_SC1);
      qb = __builtin_amdgcn_raw_buffer_load_b128(rs, qoff + 16, 0, AUX_SC1);
      if (__all(qa[1] == tag && qa[3] == tag && qb[1] == tag && qb[3] == tag)) break;
      __builtin_amdgcn_s_sleep(1);
    }
  }
  unsigned q0[4], q1[4];
  {
    const unsigned qq[4] = {qa[0], qa[2], qb[0], qb[2]};
#pragma unroll
    for (int c = 0; c < 4; ++c) { auto r = __builtin_amdgcn_permlane32_swap(qq[c], qq[c], false, false); q0[c] = r[0]; q1[c] = r[1]; }
  }
  STAMP(3);

  v4u S[DEPTH];
  float sc[4];
#pragma unroll
  for (int bt = 0; bt < WIN / 16; ++bt) {
    float x[16][2];
#pragma unroll
    for (int i = 0; i < 16; ++i) {
      const int n = bt * 16 + i;
      if (n % (WIN / DEPTH) == 0) { S[n / (WIN / DEPTH)] = ldnt(kb + SOFF(WIN + n / (WIN / DEPTH)) + lane16); SB(); }
      DOTS(x[i], R[n]);
    }
    sc[bt] = reduce_batch(x, lane);
  }
  STAMP(4);
  sc[2] = 0.f;
#pragma unroll 1
  for (int bt = WIN / 16; bt < 4; ++bt) {
    float x[16][2];
#pragma unroll
    for (int i = 0; i < 16; ++i) {
      DOTS(x[i], S[i % DEPTH]);
      const int n = bt * 16 + i + DEPTH;
      const GAS unsigned char* base = (i + DEPTH >= 16 && bt == 3) ? vb : kb;
      S[i % DEPTH] = ldnt(base + SOFF(n & 63) + lane16);
      SB();
    }
    const float v = reduce_batch(x, lane);
    sc[2] = (bt == 2) ? v : sc[2];
    sc[3] = v;
  }
  STAMP(5);
  {
    constexpr float C2 = 0.0625f * 1.4426950408889634f;
#pragma unroll
    for (int i = 0; i < 4; ++i) sc[i] = rbf(sc[i]);
    float m = fmaxf(fmaxf(sc[0], sc[1]), fmaxf(sc[2], sc[3]));
    m = fmaxf(m, dpp<XOR2>(m)); m = fmaxf(m, dpp<ROR4>(m)); m = fmaxf(m, dpp<ROR8>(m)); m = max16(m); m = max32(m);
    m *= C2;
    float l = 0.f;
#pragma unroll
    for (int i = 0; i < 4; ++i) { const float e = __builtin_amdgcn_exp2f(fmaf(sc[i], C2, -m)); l += e; lds[L_P + wave * 256 + i * 64 + lane] = e; }
    l += dpp<XOR2>(l); l += dpp<ROR4>(l); l += dpp<ROR8>(l); l = tr16(l, l); l = tr32(l, l);
    if (lane < 2) *(f32x2*)&lds[L_ML + wave * 4 + lane * 2] = f32x2{m, l};
  }
  f32x2 acc[2][4];
#pragma unroll
  for (int c = 0; c < 4; ++c) acc[0][c] = acc[1][c] = f32x2{0.f, 0.f};
  const float* pl = &lds[L_P + wave * 256 + (lane >> 5) * 32];
#define PV(VV, P0, P1) do { _Pragma("unroll") for (int c_ = 0; c_ < 4; ++c_) { const f32x2 vf_ = {bf_lo((VV)[c_]), bf_hi((VV)[c_])}; \
    acc[0][c_] = __builtin_elementwise_fma(f32x2{(P0), (P0)}, vf_, acc[0][c_]); acc[1][c_] = __builtin_elementwise_fma(f32x2{(P1), (P1)}, vf_, acc[1][c_]); } } while (0)
  static_assert(DEPTH == 4, "V loop is written for a ring of 4");
#pragma unroll 1
  for (int it = 0; it < 15; ++it) {
    const f32x4 pa = *(const f32x4*)(pl + (it >> 2) * 64 + (it & 3) * 8), pb = *(const f32x4*)(pl + (it >> 2) * 64 + (it & 3) * 8 + 4);
    PV(S[0], pa[0], pa[1]); S[0] = ldnt(vb + SOFF(it * 4 + 4) + lane16); SB();
    PV(S[1], pa[2], pa[3]); S[1] = ldnt(vb + SOFF(it * 4 + 5) + lane16); SB();
    PV(S[2], pb[0], pb[1]); S[2] = ldnt(vb + SOFF(it * 4 + 6) + lane16); SB();
    PV(S[3], pb[2], pb[3]); S[3] = ldnt(vb + SOFF(it * 4 + 7) + lane16); SB();
  }
  v4u wov[4];
  {
    const int wv = wave < 7 ? wave : 0;
    const GAS unsigned char* wp = (const GAS unsigned char*)p.wo[br] + (size_t)(kvh * 512 + 32 * j) * (DM * 2) + wv * 128;
#pragma unroll
    for (int i = 0; i < 4; ++i) wov[i] = ldnt(wp + (unsigned)((4 * (lane >> 3) + i) * (DM * 2) + (lane & 7) * 16));
    SB();
  }
  {
    const f32x4 pa = *(const f32x4*)(pl + 3 * 64 + 3 * 8), pb = *(const f32x4*)(pl + 3 * 64 + 3 * 8 + 4);
    PV(S[0], pa[0], pa[1]); PV(S[1], pa[2], pa[3]); PV(S[2], pb[0], pb[1]); PV(S[3], pb[2], pb[3]);
  }
  STAMP(6);
  {
    f32x4 o0, o1;
    o0[0] = tr32(acc[0][0][0], acc[1][0][0]); o0[1] = tr32(acc[0][0][1], acc[1][0][1]);
    o0[2] = tr32(acc[0][1][0], acc[1][1][0]); o0[3] = tr32(acc[0][1][1], acc[1][1][1]);
    o1[0] = tr32(acc[0][2][0], acc[1][2][0]); o1[1] = tr32(acc[0][2][1], acc[1][2][1]);
    o1[2] = tr32(acc[0][3][0], acc[1][3][0]); o1[3] = tr32(acc[0][3][1], acc[1][3][1]);
    *(f32x4*)&lds[L_ACC + wave * 512 + lane * 8] = o0;
    *(f32x4*)&lds[L_ACC + wave * 512 + lane * 8 + 4] = o1;
  }
  __syncthreads();
  if (tid < 256) {
    const int h = tid >> 7;
    float mw[8], M = -INFINITY;
#pragma unroll
    for (int w = 0; w < 8; ++w) { mw[w] = lds[L_ML + w * 4 + h * 2]; M = fmaxf(M, mw[w]); }
    float L = 0.f; f32x2 o = {0.f, 0.f};
#pragma unroll
    for (int w = 0; w < 8; ++w) {
      const float f = __builtin_amdgcn_exp2f(mw[w] - M);
      L = fmaf(f, lds[L_ML + w * 4 + h * 2 + 1], L);
      const f32x2 a = *(const f32x2*)&lds[L_ACC + w * 512 + 2 * tid];
      o[0] = fmaf(f, a[0], o[0]); o[1] = fmaf(f, a[1], o[1]);
    }
    const v4u gr = {f2u(o[0]), tag, f2u(o[1]), tag};
    __builtin_amdgcn_raw_buffer_store_b128(gr, rs, WS_P1 + wg * P1_STRIDE + tid * 16, 0, AUX_SC1);
    if ((tid & 127) == 0) {
      const v4u st = {f2u(M), tag, f2u(L), tag};
      __builtin_amdgcn_raw_buffer_store_b128(st, rs, WS_P1 + wg * P1_STRIDE + 4096 + h * 16, 0, AUX_SC1);
    }
  }
  STAMP(7);
  if (wave >= 6) {
    const int bb = wave - 6, sp = lane & 7, rg = lane >> 3;
    const int src = WS_P1 + (((br * 2 + bb) * 8 + kvh) * 8 + sp) * P1_STRIDE;
    const int doff = src + (32 * j + 4 * rg) * 8, soff = src + 4096 + (j >> 3) * 16;
    v4u ga, gb, gs;
    for (unsigned spins = 0;; ++spins) {
      asm volatile("" ::: "memory");
      ga = __builtin_amdgcn_raw_buffer_load_b128(rs, doff, 0, AUX_SC1);
      gb = __builtin_amdgcn_raw_buffer_load_b128(rs, doff + 16, 0, AUX_SC1);
      gs = __builtin_amdgcn_raw_buffer_load_b128(rs, soff, 0, AUX_SC1);
      if (__all(ga[1] == tag && ga[3] == tag && gb[1] == tag && gb[3] == tag && gs[1] == tag && gs[3] == tag) || spins >= SPIN_CAP) break;
      __builtin_amdgcn_s_sleep(1);
    }
    const float ms = u2f(gs[0]), ls = u2f(gs[2]);
    float M = fmaxf(ms, dpp<XOR1>(ms)); M = fmaxf(M, dpp<XOR2>(M)); M = fmaxf(M, dpp<XOR7>(M));
    const float f = __builtin_amdgcn_exp2f(ms - M);
    float L = f * ls; L += dpp<XOR1>(L); L += dpp<XOR2>(L); L += dpp<XOR7>(L);
    float o[4] = {f * u2f(ga[0]), f * u2f(ga[2]), f * u2f(gb[0]), f * u2f(gb[2])};
    const float inv = 1.f / L;
#pragma unroll
    for (int i = 0; i < 4; ++i) { o[i] += dpp<XOR1>(o[i]); o[i] += dpp<XOR2>(o[i]); o[i] += dpp<XOR7>(o[i]); o[i] = rbf(o[i] * inv); }
    if (sp == 0) *(f32x4*)&lds[L_O + bb * 32 + 4 * rg] = f32x4{o[0], o[1], o[2], o[3]};
  }
  __syncthreads();
  STAMP(8);
  if (wave < 7) {
    const f32x4 oa = *(const f32x4*)&lds[L_O + 4 * (lane >> 3)], ob = *(const f32x4*)&lds[L_O + 32 + 4 * (lane >> 3)];
    float a[2][8];
#pragma unroll
    for (int k = 0; k < 8; ++k) a[0][k] = a[1][k] = 0.f;
#pragma unroll
    for (int i = 0; i < 4; ++i)
#pragma unroll
      for (int c = 0; c < 4; ++c) {
        const float lo = bf_lo(wov[i][c]), hi = bf_hi(wov[i][c]);
        a[0][2 * c] = fmaf(oa[i], lo, a[0][2 * c]); a[0][2 * c + 1] = fmaf(oa[i], hi, a[0][2 * c + 1]);
        a[1][2 * c] = fmaf(ob[i], lo, a[1][2 * c]); a[1][2 * c + 1] = fmaf(ob[i], hi, a[1][2 * c + 1]);
      }
    float y[8], z[4];
#pragma unroll
    for (int k = 0; k < 8; ++k) y[k] = tr32(a[0][k], a[1][k]);
#pragma unroll
    for (int k = 0; k < 4; ++k) z[k] = tr16(y[k], y[k + 4]);
    const float u0 = trd<ROR8>(z[0], z[2], lane & 8), u1 = trd<ROR8>(z[1], z[3], lane & 8);
    const int oi = (lane >> 5) * DM + 64 * wave + 8 * (lane & 7) + ((lane >> 4) & 1) * 4 + ((lane >> 3) & 1) * 2;
    const v4u gr = {f2u(u0), tag, f2u(u1), tag};
    __builtin_amdgcn_raw_buffer_store_b128(gr, rs, WS_P2 + wg * P2_STRIDE + oi * 8, 0, AUX_SC1);
  }
  STAMP(9);
  if (g < 28) {
    const int off = WS_P2 + (tid >> 1) * P2_STRIDE + rid * 32 + (tid & 1) * 16;
    v4u gy;
    for (unsigned spins = 0;; ++spins) {
      asm volatile("" ::: "memory");
      gy = __builtin_amdgcn_raw_buffer_load_b128(rs, off, 0, AUX_SC1);
      if (__all(gy[1] == tag && gy[3] == tag) || spins >= SPIN_CAP) break;
      __builtin_amdgcn_s_sleep(1);
    }
    float y = trd<XOR2>(u2f(gy[0]), u2f(gy[2]), lane & 2);
    y += dpp<ROR4>(y); y += dpp<ROR8>(y); y = tr16(y, y); y = tr32(y, y);
    if (lane < 4) lds[L_RED + wave * 4 + (lane & 1) * 2 + (lane >> 1)] = y;
    __syncthreads();
    if (tid == 0) {
      f32x4 a = *(const f32x4*)&lds[L_RED], bsum = *(const f32x4*)&lds[L_RED + 16];
#pragma unroll
      for (int w = 1; w < 4; ++w) { a += *(const f32x4*)&lds[L_RED + 4 * w]; bsum += *(const f32x4*)&lds[L_RED + 16 + 4 * w]; }
      const float gt = lds[L_G], og = rbf(1.f - gt);
      unsigned short r[4];
#pragma unroll
      for (int i = 0; i < 4; ++i) r[i] = f2bf(rbf(gt * rbf(a[i])) + rbf(og * rbf(bsum[i])));
      *(GAS v2u*)((GAS unsigned char*)p.out + rid * 8) = v2u{(unsigned)r[0] | ((unsigned)r[1] << 16), (unsigned)r[2] | ((unsigned)r[3] << 16)};
      if (wg == 0) __builtin_amdgcn_raw_buffer_store_b32(tag, rs, WS_EPOCH, 0, AUX_SC1);
    }
  }
  if (br == 0) { while (__builtin_amdgcn_s_memrealtime() < t_k + 10000ull) __builtin_amdgcn_s_sleep(2); }
}

extern "C" void kernel_launch(void* const* d_in, const int* in_sizes, int n_in, void* d_out, int out_size, void* d_ws, size_t ws_size,
                              hipStream_t stream) {
  Params p;
  p.x = (const uint16_t*)d_in[0];
  p.k[0] = (const uint16_t*)d_in[1]; p.v[0] = (const uint16_t*)d_in[2]; p.wq[0] = (const uint16_t*)d_in[3]; p.wo[0] = (const uint16_t*)d_in[4];
  p.k[1] = (const uint16_t*)d_in[6]; p.v[1] = (const uint16_t*)d_in[7]; p.wq[1] = (const uint16_t*)d_in[8]; p.wo[1] = (const uint16_t*)d_in[9];
  p.gw = (const uint16_t*)d_in[10];
  p.out = (uint16_t*)d_out;
  p.ws = (unsigned char*)d_ws;
  hipLaunchKernelGGL(gqa2_fused, dim3(NWG), dim3(NTHR), 0, stream, p);
}
```

```cpp
#include <hip/hip_runtime.h>
#include <hip/hip_bf16.h>
#include <stdint.h>
#include <stdio.h>

typedef unsigned v4u __attribute__((ext_vector_type(4)));
typedef unsigned v2u __attribute__((ext_vector_type(2)));
typedef float f32x2 __attribute__((ext_vector_type(2)));
typedef float f32x4 __attribute__((ext_vector_type(4)));
typedef __bf16 bf16x2 __attribute__((ext_vector_type(2)));
#define GAS __attribute__((address_space(1)))

#define STAMPS 0

constexpr int DM = 448, NQ = 4096;
constexpr int NWG = 256, NTHR = 512;
constexpr int WIN = 32;
constexpr int DEPTH = 4;
constexpr int ROT_KB = 136;
constexpr unsigned SPIN_CAP = 1u << 16;

constexpr int WS_EPOCH = 0;
constexpr int WS_QG = 4096;
constexpr int P1_STRIDE = 4096 + 128;
constexpr int WS_P1 = WS_QG + 32 * 2048;
constexpr int P2_STRIDE = 896 * 8;
constexpr int WS_P2 = WS_P1 + NWG * P1_STRIDE;
constexpr int WS_END = WS_P2 + NWG * P2_STRIDE;
constexpr int WS_STAMP = (WS_END + 4095) & ~4095;

constexpr int L_P = 0;
constexpr int L_ACC = L_P + 8 * 256;
constexpr int L_ML = L_ACC + 8 * 512;
constexpr int L_QP = L_ML + 32;
constexpr int L_O = L_QP + 7 * 64;
constexpr int L_RED = L_O + 64;
constexpr int L_G = L_RED + 32;
constexpr int L_TOTAL = L_G + 4;

struct Params {
  const uint16_t* x;
  const uint16_t* k[2];
  const uint16_t* v[2];
  const uint16_t* wq[2];
  const uint16_t* wo[2];
  const uint16_t* gw;
  uint16_t* out;
  unsigned char* ws;
};

__device__ __forceinline__ float u2f(unsigned u) { return __builtin_bit_cast(float, u); }
__device__ __forceinline__ unsigned f2u(float f) { return __builtin_bit_cast(unsigned, f); }
__device__ __forceinline__ float dot2(unsigned k, unsigned q, float acc) {
  return __builtin_amdgcn_fdot2_f32_bf16(__builtin_bit_cast(bf16x2, k), __builtin_bit_cast(bf16x2, q), acc, false);
}
__device__ __forceinline__ float bf_lo(unsigned u) { return u2f(u << 16); }
__device__ __forceinline__ float bf_hi(unsigned u) { return u2f(u & 0xffff0000u); }
__device__ __forceinline__ unsigned short f2bf(float f) { return __builtin_bit_cast(unsigned short, (__bf16)f); }
__device__ __forceinline__ float rbf(float f) { return (float)(__bf16)f; }
template <int CTRL> __device__ __forceinline__ float dpp(float x) {
  return u2f(__builtin_amdgcn_mov_dpp(__builtin_bit_cast(int, x), CTRL, 0xf, 0xf, true));
}
constexpr int XOR1 = 0xB1, XOR2 = 0x4E, XOR7 = 0x141, ROR8 = 0x128, ROR4 = 0x124;
__device__ __forceinline__ float tr16(float a, float b) {
  auto r = __builtin_amdgcn_permlane16_swap(f2u(a), f2u(b), false, false);
  return u2f(r[0]) + u2f(r[1]);
}
__device__ __forceinline__ float tr32(float a, float b) {
  auto r = __builtin_amdgcn_permlane32_swap(f2u(a), f2u(b), false, false);
  return u2f(r[0]) + u2f(r[1]);
}
template <int CTRL> __device__ __forceinline__ float trd(float a, float b, bool bit) {
  const float t1 = a + dpp<CTRL>(a), t2 = b + dpp<CTRL>(b);
  return bit ? t2 : t1;
}
__device__ __forceinline__ float max16(float x) {
  auto r = __builtin_amdgcn_permlane16_swap(f2u(x), f2u(x), false, false);
  return fmaxf(u2f(r[0]), u2f(r[1]));
}
__device__ __forceinline__ float max32(float x) {
  auto r = __builtin_amdgcn_permlane32_swap(f2u(x), f2u(x), false, false);
  return fmaxf(u2f(r[0]), u2f(r[1]));
}
__device__ __forceinline__ v4u ldnt(const GAS unsigned char* p) { return __builtin_nontemporal_load((const GAS v4u*)p); }
#define SB() __builtin_amdgcn_sched_barrier(0)
#define PACE() asm volatile("s_waitcnt vmcnt(%0)" ::"i"(DEPTH - 1) : "memory")
constexpr int AUX_SC1 = 16;

__device__ __forceinline__ float reduce_batch(const float (&x)[16][2], int lane) {
  float y[8][2], z[4][2], u[2][2];
#pragma unroll
  for (int i = 0; i < 8; ++i) { y[i][0] = tr16(x[i][0], x[i + 8][0]); y[i][1] = tr16(x[i][1], x[i + 8][1]); }
#pragma unroll
  for (int i = 0; i < 4; ++i) { z[i][0] = trd<ROR8>(y[i][0], y[i + 4][0], lane & 8); z[i][1] = trd<ROR8>(y[i][1], y[i + 4][1], lane & 8); }
#pragma unroll
  for (int i = 0; i < 2; ++i) { u[i][0] = trd<XOR7>(z[i][0], z[i + 2][0], lane & 4); u[i][1] = trd<XOR7>(z[i][1], z[i + 2][1], lane & 4); }
  const float w0 = trd<XOR2>(u[0][0], u[1][0], lane & 2), w1 = trd<XOR2>(u[0][1], u[1][1], lane & 2);
  return trd<XOR1>(w0, w1, lane & 1);
}
#define DOTS(X, KV) do { float d0_ = 0.f, d1_ = 0.f; \
    _Pragma("unroll") for (int c_ = 0; c_ < 4; ++c_) { d0_ = dot2((KV)[c_], q0[c_], d0_); d1_ = dot2((KV)[c_], q1[c_], d1_); } \
    (X)[0] = d0_; (X)[1] = d1_; } while (0)

#define STAMP(i) do { if ((i) == 0) t_k = __builtin_amdgcn_s_memrealtime(); } while (0)
#define STAMPW(i) do { if ((i) == 0) { asm volatile("s_waitcnt vmcnt(4)" ::: "memory"); t_k = __builtin_amdgcn_s_memrealtime(); SB(); } } while (0)

__global__ void __launch_bounds__(NTHR, 2) gqa2_fused(Params p) {
  __shared__ __attribute__((aligned(16))) float lds[L_TOTAL];
  const int wg = blockIdx.x, tid = threadIdx.x, lane = tid & 63, wave = __builtin_amdgcn_readfirstlane(tid >> 6);
  const int g = wg >> 3, s = wg & 7, br = g >> 4, b = (g >> 3) & 1, kvh = g & 7, j = 2 * s + b;
  const __amdgpu_buffer_rsrc_t rs = __builtin_amdgcn_make_buffer_rsrc((void*)p.ws, 0, WS_END, 0x00020000);
  const unsigned lane16 = lane * 16u;
  unsigned long long t_k = 0;
  STAMP(0);

  const unsigned ep = __builtin_amdgcn_raw_buffer_load_b32(rs, WS_EPOCH, 0, AUX_SC1);
  v4u wqv[4];
  v2u xv[2];
  const int rid = 28 * s + g;
  if (wave < 7) {
    const int row0 = 64 * wave + 4 * (lane >> 2);
    const GAS unsigned char* wp = (const GAS unsigned char*)p.wq[br] + (size_t)(kvh * 512 + 32 * j) * 2;
#pragma unroll
    for (int i = 0; i < 4; ++i) wqv[i] = ldnt(wp + (unsigned)((row0 + i) * (NQ * 2) + (lane & 3) * 16));
    xv[0] = *(const GAS v2u*)((const GAS unsigned char*)p.x + row0 * 2);
    xv[1] = *(const GAS v2u*)((const GAS unsigned char*)p.x + (DM + row0) * 2);
  } else {
    const int l = lane < 56 ? lane : 0, bo = (4 * rid >= DM) ? 1 : 0;
    wqv[0] = ldnt((const GAS unsigned char*)p.x + bo * (DM * 2) + l * 16);
    wqv[1] = ldnt((const GAS unsigned char*)p.gw + l * 16);
    wqv[2] = wqv[3] = v4u{0u, 0u, 0u, 0u};
    xv[0] = xv[1] = v2u{0u, 0u};
  }
  const GAS unsigned char* kb = (const GAS unsigned char*)p.k[br] + (size_t)(g & 15) * (4u << 20);
  const GAS unsigned char* vb = (const GAS unsigned char*)p.v[br] + (size_t)(g & 15) * (4u << 20);
  const unsigned kb0 = (unsigned)((s * 8 + wave) * 2 + g * ROT_KB);
#define SOFF(n) ((((kb0 + ((n) >> 1) * 128u) & 4095u) + ((n) & 1)) << 10)
  v4u R[WIN];
#pragma unroll
  for (int i = 0; i < DEPTH; ++i) R[i] = ldnt(kb + SOFF(i) + lane16);
  SB();

  STAMPW(11);
  if (wave < 7) {
    float a[2][8];
#pragma unroll
    for (int k = 0; k < 8; ++k) a[0][k] = a[1][k] = 0.f;
#pragma unroll
    for (int i = 0; i < 4; ++i) {
      const float x0 = (i & 1) ? bf_hi(xv[0][i >> 1]) : bf_lo(xv[0][i >> 1]);
      const float x1 = (i & 1) ? bf_hi(xv[1][i >> 1]) : bf_lo(xv[1][i >> 1]);
#pragma unroll
      for (int c = 0; c < 4; ++c) {
        const float lo = bf_lo(wqv[i][c]), hi = bf_hi(wqv[i][c]);
        a[0][2 * c] = fmaf(x0, lo, a[0][2 * c]); a[0][2 * c + 1] = fmaf(x0, hi, a[0][2 * c + 1]);
        a[1][2 * c] = fmaf(x1, lo, a[1][2 * c]); a[1][2 * c + 1] = fmaf(x1, hi, a[1][2 * c + 1]);
      }
    }
    float y[8], z[4], u[2];
#pragma unroll
    for (int k = 0; k < 8; ++k) y[k] = tr32(a[0][k], a[1][k]);
#pragma unroll
    for (int k = 0; k < 4; ++k) z[k] = tr16(y[k], y[k + 4]);
#pragma unroll
    for (int k = 0; k < 2; ++k) u[k] = trd<ROR8>(z[k], z[k + 2], lane & 8);
    const float keep = (lane & 4) ? u[1] : u[0], send = (lane & 4) ? u[0] : u[1];
    const float o4a = dpp<0x124>(send), o4b = dpp<0x12C>(send);
    const float o4 = (lane & 4) ? o4a : o4b;
    const float r = keep + o4;
    lds[L_QP + wave * 64 + (lane >> 5) * 32 + (lane & 3) * 8 + ((lane >> 2) & 7)] = r;
  } else {
    float z = 0.f;
#pragma unroll
    for (int c = 0; c < 4; ++c) z += rbf(bf_lo(wqv[0][c]) * bf_lo(wqv[1][c])) + rbf(bf_hi(wqv[0][c]) * bf_hi(wqv[1][c]));
    if (lane >= 56) z = 0.f;
    z += dpp<XOR1>(z); z += dpp<XOR2>(z); z += dpp<ROR4>(z); z += dpp<ROR8>(z);
    z = tr16(z, z); z = tr32(z, z);
    z = rbf(z);
    if (lane == 0) lds[L_G] = rbf(1.f / (1.f + __expf(-z)));
  }
  const unsigned tag = __builtin_amdgcn_readfirstlane(ep) + 1u;
  __syncthreads();
  if (wave == 0 && lane < 16) {
    f32x4 q4 = *(const f32x4*)&lds[L_QP + 4 * lane];
#pragma unroll
    for (int w = 1; w < 7; ++w) q4 += *(const f32x4*)&lds[L_QP + w * 64 + 4 * lane];
    const v4u gr = {(unsigned)f2bf(q4[0]) | ((unsigned)f2bf(q4[1]) << 16), tag, (unsigned)f2bf(q4[2]) | ((unsigned)f2bf(q4[3]) << 16), tag};
    __builtin_amdgcn_raw_buffer_store_b128(gr, rs, WS_QG + ((br * 8 + kvh) * 2 + (lane >> 3)) * 2048 + j * 128 + (lane & 7) * 16, 0, AUX_SC1);
  }
  STAMP(1);

  const int qoff = WS_QG + ((br * 8 + kvh) * 2 + b) * 2048 + lane * 32;
  v4u qa, qb;
#pragma unroll
  for (int i = DEPTH; i < WIN; ++i) {
    PACE();
    R[i] = ldnt(kb + SOFF(i) + lane16);
    if (i == 8) STAMP(12);
    if (i == 16) STAMP(13);
    if (i == WIN - 6) {
      qa = __builtin_amdgcn_raw_buffer_load_b128(rs, qoff, 0, AUX_SC1);
      qb = __builtin_amdgcn_raw_buffer_load_b128(rs, qoff + 16, 0, AUX_SC1);
    }
    SB();
  }
  STAMP(2);
  if (__builtin_expect(!__all(qa[1] == tag && qa[3] == tag && qb[1] == tag && qb[3] == tag), 0)) {
    for (unsigned spins = 0; spins < SPIN_CAP; ++spins) {
      asm volatile("" ::: "memory");
      qa = __builtin_amdgcn_raw_buffer_load_b128(rs, qoff, 0, AUX_SC1);
      qb = __builtin_amdgcn_raw_buffer_load_b128(rs, qoff + 16, 0, AUX_SC1);
      if (__all(qa[1] == tag && qa[3] == tag && qb[1] == tag && qb[3] == tag)) break;
      __builtin_amdgcn_s_sleep(1);
    }
  }
  unsigned q0[4], q1[4];
  {
    const unsigned qq[4] = {qa[0], qa[2], qb[0], qb[2]};
#pragma unroll
    for (int c = 0; c < 4; ++c) { auto r = __builtin_amdgcn_permlane32_swap(qq[c], qq[c], false, false); q0[c] = r[0]; q1[c] = r[1]; }
  }
  STAMP(3);

  v4u S[DEPTH];
  float sc[4];
#pragma unroll
  for (int bt = 0; bt < WIN / 16; ++bt) {
    float x[16][2];
#pragma unroll
    for (int i = 0; i < 16; ++i) {
      const int n = bt * 16 + i;
      if (n % (WIN / DEPTH) == 0) { S[n / (WIN / DEPTH)] = ldnt(kb + SOFF(WIN + n / (WIN / DEPTH)) + lane16); SB(); }
      DOTS(x[i], R[n]);
    }
    sc[bt] = reduce_batch(x, lane);
  }
  STAMP(4);
  sc[2] = 0.f;
#pragma unroll 1
  for (int bt = WIN / 16; bt < 4; ++bt) {
    float x[16][2];
#pragma unroll
    for (int i = 0; i < 16; ++i) {
      DOTS(x[i], S[i % DEPTH]);
      const int n = bt * 16 + i + DEPTH;
      const GAS unsigned char* base = (i + DEPTH >= 16 && bt == 3) ? vb : kb;
      S[i % DEPTH] = ldnt(base + SOFF(n & 63) + lane16);
      SB();
    }
    const float v = reduce_batch(x, lane);
    sc[2] = (bt == 2) ? v : sc[2];
    sc[3] = v;
  }
  STAMP(5);
  {
    constexpr float C2 = 0.0625f * 1.4426950408889634f;
#pragma unroll
    for (int i = 0; i < 4; ++i) sc[i] = rbf(sc[i]);
    float m = fmaxf(fmaxf(sc[0], sc[1]), fmaxf(sc[2], sc[3]));
    m = fmaxf(m, dpp<XOR2>(m)); m = fmaxf(m, dpp<ROR4>(m)); m = fmaxf(m, dpp<ROR8>(m)); m = max16(m); m = max32(m);
    m *= C2;
    float l = 0.f;
#pragma unroll
    for (int i = 0; i < 4; ++i) { const float e = __builtin_amdgcn_exp2f(fmaf(sc[i], C2, -m)); l += e; lds[L_P + wave * 256 + i * 64 + lane] = e; }
    l += dpp<XOR2>(l); l += dpp<ROR4>(l); l += dpp<ROR8>(l); l = tr16(l, l); l = tr32(l, l);
    if (lane < 2) *(f32x2*)&lds[L_ML + wave * 4 + lane * 2] = f32x2{m, l};
  }
  f32x2 acc[2][4];
#pragma unroll
  for (int c = 0; c < 4; ++c) acc[0][c] = acc[1][c] = f32x2{0.f, 0.f};
  const float* pl = &lds[L_P + wave * 256 + (lane >> 5) * 32];
#define PV(VV, P0, P1) do { _Pragma("unroll") for (int c_ = 0; c_ < 4; ++c_) { const f32x2 vf_ = {bf_lo((VV)[c_]), bf_hi((VV)[c_])}; \
    acc[0][c_] = __builtin_elementwise_fma(f32x2{(P0), (P0)}, vf_, acc[0][c_]); acc[1][c_] = __builtin_elementwise_fma(f32x2{(P1), (P1)}, vf_, acc[1][c_]); } } while (0)
  static_assert(DEPTH == 4, "V loop is written for a ring of 4");
#pragma unroll 1
  for (int it = 0; it < 15; ++it) {
    const f32x4 pa = *(const f32x4*)(pl + (it >> 2) * 64 + (it & 3) * 8), pb = *(const f32x4*)(pl + (it >> 2) * 64 + (it & 3) * 8 + 4);
    PV(S[0], pa[0], pa[1]); S[0] = ldnt(vb + SOFF(it * 4 + 4) + lane16); SB();
    PV(S[1], pa[2], pa[3]); S[1] = ldnt(vb + SOFF(it * 4 + 5) + lane16); SB();
    PV(S[2], pb[0], pb[1]); S[2] = ldnt(vb + SOFF(it * 4 + 6) + lane16); SB();
    PV(S[3], pb[2], pb[3]); S[3] = ldnt(vb + SOFF(it * 4 + 7) + lane16); SB();
  }
  v4u wov[4];
  {
    const int wv = wave < 7 ? wave : 0;
    const GAS unsigned char* wp = (const GAS unsigned char*)p.wo[br] + (size_t)(kvh * 512 + 32 * j) * (DM * 2) + wv * 128;
#pragma unroll
    for (int i = 0; i < 4; ++i) wov[i] = ldnt(wp + (unsigned)((4 * (lane >> 3) + i) * (DM * 2) + (lane & 7) * 16));
    SB();
  }
  {
    const f32x4 pa = *(const f32x4*)(pl + 3 * 64 + 3 * 8), pb = *(const f32x4*)(pl + 3 * 64 + 3 * 8 + 4);
    PV(S[0], pa[0], pa[1]); PV(S[1], pa[2], pa[3]); PV(S[2], pb[0], pb[1]); PV(S[3], pb[2], pb[3]);
  }
  STAMP(6);
  {
    f32x4 o0, o1;
    o0[0] = tr32(acc[0][0][0], acc[1][0][0]); o0[1] = tr32(acc[0][0][1], acc[1][0][1]);
    o0[2] = tr32(acc[0][1][0], acc[1][1][0]); o0[3] = tr32(acc[0][1][1], acc[1][1][1]);
    o1[0] = tr32(acc[0][2][0], acc[1][2][0]); o1[1] = tr32(acc[0][2][1], acc[1][2][1]);
    o1[2] = tr32(acc[0][3][0], acc[1][3][0]); o1[3] = tr32(acc[0][3][1], acc[1][3][1]);
    *(f32x4*)&lds[L_ACC + wave * 512 + lane * 8] = o0;
    *(f32x4*)&lds[L_ACC + wave * 512 + lane * 8 + 4] = o1;
  }
  __syncthreads();
  if (tid < 256) {
    const int h = tid >> 7;
    float mw[8], M = -INFINITY;
#pragma unroll
    for (int w = 0; w < 8; ++w) { mw[w] = lds[L_ML + w * 4 + h * 2]; M = fmaxf(M, mw[w]); }
    float L = 0.f; f32x2 o = {0.f, 0.f};
#pragma unroll
    for (int w = 0; w < 8; ++w) {
      const float f = __builtin_amdgcn_exp2f(mw[w] - M);
      L = fmaf(f, lds[L_ML + w * 4 + h * 2 + 1], L);
      const f32x2 a = *(const f32x2*)&lds[L_ACC + w * 512 + 2 * tid];
      o[0] = fmaf(f, a[0], o[0]); o[1] = fmaf(f, a[1], o[1]);
    }
    const v4u gr = {f2u(o[0]), tag, f2u(o[1]), tag};
    __builtin_amdgcn_raw_buffer_store_b128(gr, rs, WS_P1 + wg * P1_STRIDE + tid * 16, 0, AUX_SC1);
    if ((tid & 127) == 0) {
      const v4u st = {f2u(M), tag, f2u(L), tag};
      __builtin_amdgcn_raw_buffer_store_b128(st, rs, WS_P1 + wg * P1_STRIDE + 4096 + h * 16, 0, AUX_SC1);
    }
  }
  STAMP(7);
  if (wave >= 6) {
    const int bb = wave - 6, sp = lane & 7, rg = lane >> 3;
    const int src = WS_P1 + (((br * 2 + bb) * 8 + kvh) * 8 + sp) * P1_STRIDE;
    const int doff = src + (32 * j + 4 * rg) * 8, soff = src + 4096 + (j >> 3) * 16;
    v4u ga, gb, gs;
    for (unsigned spins = 0;; ++spins) {
      asm volatile("" ::: "memory");
      ga = __builtin_amdgcn_raw_buffer_load_b128(rs, doff, 0, AUX_SC1);
      gb = __builtin_amdgcn_raw_buffer_load_b128(rs, doff + 16, 0, AUX_SC1);
      gs = __builtin_amdgcn_raw_buffer_load_b128(rs, soff, 0, AUX_SC1);
      if (__all(ga[1] == tag && ga[3] == tag && gb[1] == tag && gb[3] == tag && gs[1] == tag && gs[3] == tag) || spins >= SPIN_CAP) break;
      __builtin_amdgcn_s_sleep(1);
    }
    const float ms = u2f(gs[0]), ls = u2f(gs[2]);
    float M = fmaxf(ms, dpp<XOR1>(ms)); M = fmaxf(M, dpp<XOR2>(M)); M = fmaxf(M, dpp<XOR7>(M));
    const float f = __builtin_amdgcn_exp2f(ms - M);
    float L = f * ls; L += dpp<XOR1>(L); L += dpp<XOR2>(L); L += dpp<XOR7>(L);
    float o[4] = {f * u2f(ga[0]), f * u2f(ga[2]), f * u2f(gb[0]), f * u2f(gb[2])};
    const float inv = 1.f / L;
#pragma unroll
    for (int i = 0; i < 4; ++i) { o[i] += dpp<XOR1>(o[i]); o[i] += dpp<XOR2>(o[i]); o[i] += dpp<XOR7>(o[i]); o[i] = rbf(o[i] * inv); }
    if (sp == 0) *(f32x4*)&lds[L_O + bb * 32 + 4 * rg] = f32x4{o[0], o[1], o[2], o[3]};
  }
  __syncthreads();
  STAMP(8);
  if (wave < 7) {
    const f32x4 oa = *(const f32x4*)&lds[L_O + 4 * (lane >> 3)], ob = *(const f32x4*)&lds[L_O + 32 + 4 * (lane >> 3)];
    float a[2][8];
#pragma unroll
    for (int k = 0; k < 8; ++k) a[0][k] = a[1][k] = 0.f;
#pragma unroll
    for (int i = 0; i < 4; ++i)
#pragma unroll
      for (int c = 0; c < 4; ++c) {
        const float lo = bf_lo(wov[i][c]), hi = bf_hi(wov[i][c]);
        a[0][2 * c] = fmaf(oa[i], lo, a[0][2 * c]); a[0][2 * c + 1] = fmaf(oa[i], hi, a[0][2 * c + 1]);
        a[1][2 * c] = fmaf(ob[i], lo, a[1][2 * c]); a[1][2 * c + 1] = fmaf(ob[i], hi, a[1][2 * c + 1]);
      }
    float y[8], z[4];
#pragma unroll
    for (int k = 0; k < 8; ++k) y[k] = tr32(a[0][k], a[1][k]);
#pragma unroll
    for (int k = 0; k < 4; ++k) z[k] = tr16(y[k], y[k + 4]);
    const float u0 = trd<ROR8>(z[0], z[2], lane & 8), u1 = trd<ROR8>(z[1], z[3], lane & 8);
    const int oi = (lane >> 5) * DM + 64 * wave + 8 * (lane & 7) + ((lane >> 4) & 1) * 4 + ((lane >> 3) & 1) * 2;
    const v4u gr = {f2u(u0), tag, f2u(u1), tag};
    __builtin_amdgcn_raw_buffer_store_b128(gr, rs, WS_P2 + wg * P2_STRIDE + oi * 8, 0, AUX_SC1);
  }
  STAMP(9);
  if (g < 28) {
    const int off = WS_P2 + (tid >> 1) * P2_STRIDE + rid * 32 + (tid & 1) * 16;
    v4u gy;
    for (unsigned spins = 0;; ++spins) {
      asm volatile("" ::: "memory");
      gy = __builtin_amdgcn_raw_buffer_load_b128(rs, off, 0, AUX_SC1);
      if (__all(gy[1] == tag && gy[3] == tag) || spins >= SPIN_CAP) break;
      __builtin_amdgcn_s_sleep(1);
    }
    float y = trd<XOR2>(u2f(gy[0]), u2f(gy[2]), lane & 2);
    y += dpp<ROR4>(y); y += dpp<ROR8>(y); y = tr16(y, y); y = tr32(y, y);
    if (lane < 4) lds[L_RED + wave * 4 + (lane & 1) * 2 + (lane >> 1)] = y;
    __syncthreads();
    if (tid == 0) {
      f32x4 a = *(const f32x4*)&lds[L_RED], bsum = *(const f32x4*)&lds[L_RED + 16];
#pragma unroll
      for (int w = 1; w < 4; ++w) { a += *(const f32x4*)&lds[L_RED + 4 * w]; bsum += *(const f32x4*)&lds[L_RED + 16 + 4 * w]; }
      const float gt = lds[L_G], og = rbf(1.f - gt);
      unsigned short r[4];
#pragma unroll
      for (int i = 0; i < 4; ++i) r[i] = f2bf(rbf(gt * rbf(a[i])) + rbf(og * rbf(bsum[i])));
      *(GAS v2u*)((GAS unsigned char*)p.out + rid * 8) = v2u{(unsigned)r[0] | ((unsigned)r[1] << 16), (unsigned)r[2] | ((unsigned)r[3] << 16)};
      if (wg == 0) __builtin_amdgcn_raw_buffer_store_b32(tag, rs, WS_EPOCH, 0, AUX_SC1);
    }
  }
  if (wg == 255) { while (__builtin_amdgcn_s_memrealtime() < t_k + 10000ull) __builtin_amdgcn_s_sleep(2); }
}

extern "C" void kernel_launch(void* const* d_in, const int* in_sizes, int n_in, void* d_out, int out_size, void* d_ws, size_t ws_size,
                              hipStream_t stream) {
  Params p;
  p.x = (const uint16_t*)d_in[0];
  p.k[0] = (const uint16_t*)d_in[1]; p.v[0] = (const uint16_t*)d_in[2]; p.wq[0] = (const uint16_t*)d_in[3]; p.wo[0] = (const uint16_t*)d_in[4];
  p.k[1] = (const uint16_t*)d_in[6]; p.v[1] = (const uint16_t*)d_in[7]; p.wq[1] = (const uint16_t*)d_in[8]; p.wo[1] = (const uint16_t*)d_in[9];
  p.gw = (const uint16_t*)d_in[10];
  p.out = (uint16_t*)d_out;
  p.ws = (unsigned char*)d_ws;
  hipLaunchKernelGGL(gqa2_fused, dim3(NWG), dim3(NTHR), 0, stream, p);
}
```

```cpp
#include <hip/hip_runtime.h>
#include <hip/hip_bf16.h>
#include <stdint.h>

typedef unsigned v4u __attribute__((ext_vector_type(4)));
typedef unsigned v2u __attribute__((ext_vector_type(2)));
typedef float f32x2 __attribute__((ext_vector_type(2)));
typedef float f32x4 __attribute__((ext_vector_type(4)));
typedef __bf16 bf16x2 __attribute__((ext_vector_type(2)));
#define GAS __attribute__((address_space(1)))
typedef const GAS unsigned char* gptr;

#define STAMPS 0
#define T_STREAM 37.0
#define ROT_KB 136
#define INFL 6
#define TB_OFF 80
#define WQ_NT 1
#define DRYRUN 0
#define SLEEP1 1
#define SLEEP2 1

constexpr int DM = 448, NQ = 4096;
constexpr int NWG = 256, NTHR = 576;
constexpr unsigned TAU16 = (unsigned)(T_STREAM * 1600.0 / 128.0 + 0.5);
constexpr unsigned SPIN_CAP = 1u << 16;

constexpr int WS_EPOCH = 0;
constexpr int WS_QG = 4096;
constexpr int P1_STRIDE = 4096 + 128;
constexpr int WS_P1 = WS_QG + 32 * 2048;
constexpr int P2_STRIDE = 896 * 8;
constexpr int WS_P2 = WS_P1 + NWG * P1_STRIDE;
constexpr int WS_END = WS_P2 + NWG * P2_STRIDE;
constexpr int WS_STAMP = (WS_END + 8191) & ~4095;

constexpr int L_P = 0;
constexpr int L_ACC = L_P + 8 * 256;
constexpr int L_M = L_ACC + 9 * 512;
constexpr int L_L = L_M + 16;
constexpr int L_QP = L_L + 16;
constexpr int L_Q = L_QP + 8 * 64;
constexpr int L_O = L_Q + 256;
constexpr int L_W = L_O + 96;
constexpr int L_RED = L_W + 16;
constexpr int L_G = L_RED + 9 * 32;
constexpr int L_TOTAL = L_G + 4;

struct Params {
  const uint16_t* k[2];
  const uint16_t* wq[2];
  const uint16_t* x;
  unsigned char* ws;
  const uint16_t* v[2];
  const uint16_t* wo[2];
  const uint16_t* gw;
  uint16_t* out;
};

__device__ __forceinline__ float u2f(unsigned u) { return __builtin_bit_cast(float, u); }
__device__ __forceinline__ unsigned f2u(float f) { return __builtin_bit_cast(unsigned, f); }
__device__ __forceinline__ float dot2(unsigned k, unsigned q, float acc) {
  return __builtin_amdgcn_fdot2_f32_bf16(__builtin_bit_cast(bf16x2, k), __builtin_bit_cast(bf16x2, q), acc, false);
}
__device__ __forceinline__ float bf_lo(unsigned u) { return u2f(u << 16); }
__device__ __forceinline__ float bf_hi(unsigned u) { return u2f(u & 0xffff0000u); }
__device__ __forceinline__ unsigned f2bf(float f) { return __builtin_bit_cast(unsigned short, (__bf16)f); }
__device__ __forceinline__ float rbf(float f) { return (float)(__bf16)f; }
template <int CTRL> __device__ __forceinline__ float dpp(float x) {
  return u2f(__builtin_amdgcn_mov_dpp(__builtin_bit_cast(int, x), CTRL, 0xf, 0xf, true));
}
constexpr int XOR1 = 0xB1, XOR2 = 0x4E, XOR7 = 0x141, ROR8 = 0x128, ROR4 = 0x124, ROR12 = 0x12C;
__device__ __forceinline__ float tr16(float a, float b) {
  auto r = __builtin_amdgcn_permlane16_swap(f2u(a), f2u(b), false, false);
  return u2f(r[0]) + u2f(r[1]);
}
__device__ __forceinline__ float tr32(float a, float b) {
  auto r = __builtin_amdgcn_permlane32_swap(f2u(a), f2u(b), false, false);
  return u2f(r[0]) + u2f(r[1]);
}
template <int CTRL> __device__ __forceinline__ float trd(float a, float b, bool bit) {
  const float t1 = a + dpp<CTRL>(a), t2 = b + dpp<CTRL>(b);
  return bit ? t2 : t1;
}
__device__ __forceinline__ float max16(float x) {
  auto r = __builtin_amdgcn_permlane16_swap(f2u(x), f2u(x), false, false);
  return fmaxf(u2f(r[0]), u2f(r[1]));
}
__device__ __forceinline__ float max32(float x) {
  auto r = __builtin_amdgcn_permlane32_swap(f2u(x), f2u(x), false, false);
  return fmaxf(u2f(r[0]), u2f(r[1]));
}
__device__ __forceinline__ v4u ldnt(gptr p) { return __builtin_nontemporal_load((const GAS v4u*)p); }
#define SB() __builtin_amdgcn_sched_barrier(0)
constexpr int AUX_SC1 = 16;
__device__ __forceinline__ void gate(unsigned due) {
  asm volatile("s_waitcnt vmcnt(%1)\n"
               "1:\n\ts_memrealtime vcc\n\ts_waitcnt lgkmcnt(0)\n\ts_sub_u32 vcc_lo, vcc_lo, %0\n\ts_cmp_lt_i32 vcc_lo, 0\n\t"
               "s_cbranch_scc0 2f\n\ts_sleep 1\n\ts_branch 1b\n2:" ::"s"(due), "i"(INFL - 2) : "vcc", "scc", "memory");
}

__device__ __forceinline__ float reduce_batch(const float (&x)[16][2], int lane) {
  float y[8][2], z[4][2], u[2][2];
#pragma unroll
  for (int i = 0; i < 8; ++i) { y[i][0] = tr16(x[i][0], x[i + 8][0]); y[i][1] = tr16(x[i][1], x[i + 8][1]); }
#pragma unroll
  for (int i = 0; i < 4; ++i) { z[i][0] = trd<ROR8>(y[i][0], y[i + 4][0], lane & 8); z[i][1] = trd<ROR8>(y[i][1], y[i + 4][1], lane & 8); }
#pragma unroll
  for (int i = 0; i < 2; ++i) { u[i][0] = trd<XOR7>(z[i][0], z[i + 2][0], lane & 4); u[i][1] = trd<XOR7>(z[i][1], z[i + 2][1], lane & 4); }
  const float w0 = trd<XOR2>(u[0][0], u[1][0], lane & 2), w1 = trd<XOR2>(u[0][1], u[1][1], lane & 2);
  return rbf(trd<XOR1>(w0, w1, lane & 1));
}
#define DOTS(X, KV) do { float d0_ = 0.f, d1_ = 0.f; \
    _Pragma("unroll") for (int c_ = 0; c_ < 4; ++c_) { d0_ = dot2((KV)[c_], q0[c_], d0_); d1_ = dot2((KV)[c_], q1[c_], d1_); } \
    (X)[0] = d0_; (X)[1] = d1_; } while (0)

#define STAMP(i) do {} while (0)
#define BAR() asm volatile("s_waitcnt lgkmcnt(0)\n\ts_barrier" ::: "memory")
constexpr float C2 = 0.0625f * 1.4426950408889634f;

__global__ void __launch_bounds__(NTHR, 3) gqa2_fused(Params p) {
  __shared__ __attribute__((aligned(16))) float lds[L_TOTAL];
  const int wg = blockIdx.x, tid = threadIdx.x, lane = tid & 63, wave = __builtin_amdgcn_readfirstlane(tid >> 6);
  const int br = wg >> 7, b = (wg >> 6) & 1, s = (wg >> 3) & 7, kvh = wg & 7, j = 2 * s + b;
  const int kvo = (kvh + 1) & 7;
  const unsigned t_entry = (unsigned)__builtin_amdgcn_s_memrealtime();
  const uint16_t *k0 = p.k[0], *k1 = p.k[1], *wq0 = p.wq[0], *wq1 = p.wq[1], *xp = p.x, *v0 = p.v[0], *v1 = p.v[1], *wo0 = p.wo[0], *wo1 = p.wo[1], *gwp = p.gw;
  unsigned char* wsp = p.ws;
  uint16_t* outp = p.out;
  asm volatile("" : "+s"(k0), "+s"(k1), "+s"(wq0), "+s"(wq1), "+s"(xp), "+s"(wsp), "+s"(v0), "+s"(v1), "+s"(wo0), "+s"(wo1), "+s"(gwp), "+s"(outp));
  const unsigned lane16 = lane * 16u;
  const bool helper = wave == 8;
  const int rid = 7 * kvh + (wg >> 3);
  unsigned tag;
  f32x2 acc[2][4];
  v4u wov[4];

  if (!helper) {
    v4u wqv[4];
    v2u xv[2];
    {
      const int row0 = 56 * wave + (lane < 56 ? 4 * (lane >> 2) : 0);
      const gptr wp = (gptr)(br ? wq1 : wq0) + (kvo * 512 + 32 * j) * 2;
#pragma unroll
      for (int i = 0; i < 4; ++i) {
        const gptr a = wp + (unsigned)((row0 + i) * (NQ * 2) + (lane & 3) * 16);
        wqv[i] = WQ_NT ? ldnt(a) : *(const GAS v4u*)a;
      }
      xv[0] = *(const GAS v2u*)((gptr)xp + row0 * 2);
      xv[1] = *(const GAS v2u*)((gptr)xp + (DM + row0) * 2);
    }
    const size_t reg = (size_t)(b * 8 + kvh) * (4u << 20);
    const gptr kb = (gptr)(br ? k1 : k0) + reg, vb = (gptr)(br ? v1 : v0) + reg;
    const unsigned kb0 = (unsigned)(s * 512 + wave * 2 + (br * 16 + b * 8 + kvh) * ROT_KB);
#define SOFF(n) ((((kb0 + ((n) >> 1) * 16u) & 4095u) + ((n) & 1)) << 10)
    v4u A[16], B[16];
#pragma unroll
    for (int i = 0; i < 8; ++i) A[i] = ldnt(kb + SOFF(i) + lane16);
    SB();
    const unsigned tb = t_entry + TB_OFF;
#define DUE(n) (tb + (((unsigned)(n) * TAU16) >> 4))
    STAMP(1);
    {
      float a[2][8];
#pragma unroll
      for (int k = 0; k < 8; ++k) a[0][k] = a[1][k] = 0.f;
      if (lane >= 56) { xv[0] = v2u{0u, 0u}; xv[1] = v2u{0u, 0u}; }
#pragma unroll
      for (int i = 0; i < 4; ++i) {
        const float x0 = (i & 1) ? bf_hi(xv[0][i >> 1]) : bf_lo(xv[0][i >> 1]);
        const float x1 = (i & 1) ? bf_hi(xv[1][i >> 1]) : bf_lo(xv[1][i >> 1]);
#pragma unroll
        for (int c = 0; c < 4; ++c) {
          const unsigned wv = lane < 56 ? wqv[i][c] : 0u;
          const float lo = bf_lo(wv), hi = bf_hi(wv);
          a[0][2 * c] = fmaf(x0, lo, a[0][2 * c]); a[0][2 * c + 1] = fmaf(x0, hi, a[0][2 * c + 1]);
          a[1][2 * c] = fmaf(x1, lo, a[1][2 * c]); a[1][2 * c + 1] = fmaf(x1, hi, a[1][2 * c + 1]);
        }
      }
      float y[8], z[4], u[2];
#pragma unroll
      for (int k = 0; k < 8; ++k) y[k] = tr32(a[0][k], a[1][k]);
#pragma unroll
      for (int k = 0; k < 4; ++k) z[k] = tr16(y[k], y[k + 4]);
#pragma unroll
      for (int k = 0; k < 2; ++k) u[k] = trd<ROR8>(z[k], z[k + 2], lane & 8);
      const float keep = (lane & 4) ? u[1] : u[0], send = (lane & 4) ? u[0] : u[1];
      const float o4a = dpp<ROR4>(send), o4b = dpp<ROR12>(send);
      lds[L_QP + wave * 64 + (lane >> 5) * 32 + (lane & 3) * 8 + ((lane >> 2) & 7)] = keep + ((lane & 4) ? o4a : o4b);
    }
    BAR();
    STAMP(2);
#pragma unroll
    for (int i = 8; i < 24; i += 2) {
      gate(DUE(i));
      if (i < 16) { A[i] = ldnt(kb + SOFF(i) + lane16); A[i + 1] = ldnt(kb + SOFF(i + 1) + lane16); }
      else { B[i - 16] = ldnt(kb + SOFF(i) + lane16); B[i - 15] = ldnt(kb + SOFF(i + 1) + lane16); }
      SB();
    }
    STAMP(3);
    BAR();
    const v4u q0 = *(const v4u*)&lds[L_Q + (lane & 31) * 4], q1 = *(const v4u*)&lds[L_Q + 128 + (lane & 31) * 4];
    tag = __builtin_amdgcn_readfirstlane(f2u(lds[L_G + 1]));
    STAMP(4);
    float sc[4];
    {
      float x[16][2];
#pragma unroll
      for (int i = 0; i < 16; i += 4) {
        gate(DUE(24 + i / 2));
        B[8 + i / 2] = ldnt(kb + SOFF(24 + i / 2) + lane16); B[9 + i / 2] = ldnt(kb + SOFF(25 + i / 2) + lane16);
        SB();
        DOTS(x[i], A[i]); DOTS(x[i + 1], A[i + 1]); DOTS(x[i + 2], A[i + 2]); DOTS(x[i + 3], A[i + 3]);
        SB();
      }
      sc[0] = reduce_batch(x, lane);
    }
    sc[1] = sc[2] = 0.f;
#pragma unroll 1
    for (int bt = 1; bt < 4; ++bt) {
      float x[16][2];
      const gptr base = (bt == 3) ? vb : kb;
#pragma unroll
      for (int i = 0; i < 16; i += 2) {
        DOTS(x[i], B[i]); DOTS(x[i + 1], B[i + 1]);
        const int n = bt * 16 + i + 16;
        SB();
        gate(DUE(n));
        B[i] = ldnt(base + SOFF(n & 63) + lane16); B[i + 1] = ldnt(base + SOFF((n & 63) + 1) + lane16);
        SB();
      }
      const float v = reduce_batch(x, lane);
      sc[1] = (bt == 1) ? v : sc[1];
      sc[2] = (bt == 2) ? v : sc[2];
      sc[3] = v;
    }
    STAMP(5);
    {
      float m = fmaxf(fmaxf(sc[0], sc[1]), fmaxf(sc[2], sc[3]));
      m = fmaxf(m, dpp<XOR2>(m)); m = fmaxf(m, dpp<ROR4>(m)); m = fmaxf(m, dpp<ROR8>(m)); m = max16(m); m = max32(m);
      if (lane < 2) lds[L_M + lane * 8 + wave] = m;
      BAR();
      const f32x4 ma = *(const f32x4*)&lds[L_M + (lane & 1) * 8], mb = *(const f32x4*)&lds[L_M + (lane & 1) * 8 + 4];
      const float Mh = fmaxf(fmaxf(fmaxf(ma[0], ma[1]), fmaxf(ma[2], ma[3])), fmaxf(fmaxf(mb[0], mb[1]), fmaxf(mb[2], mb[3]))) * C2;
      float l = 0.f;
#pragma unroll
      for (int i = 0; i < 4; ++i) { const float e = __builtin_amdgcn_exp2f(fmaf(sc[i], C2, -Mh)); l += e; lds[L_P + wave * 256 + i * 64 + lane] = e; }
      l += dpp<XOR2>(l); l += dpp<ROR4>(l); l += dpp<ROR8>(l); l = tr16(l, l); l = tr32(l, l);
      if (lane < 2) lds[L_L + lane * 8 + wave] = l;
    }
#pragma unroll
    for (int c = 0; c < 4; ++c) acc[0][c] = acc[1][c] = f32x2{0.f, 0.f};
    const float* pl = &lds[L_P + wave * 256 + (lane >> 5) * 32];
#define PV(VV, P0, P1) do { _Pragma("unroll") for (int c_ = 0; c_ < 4; ++c_) { const f32x2 vf_ = {bf_lo((VV)[c_]), bf_hi((VV)[c_])}; \
    acc[0][c_] = __builtin_elementwise_fma(f32x2{(P0), (P0)}, vf_, acc[0][c_]); acc[1][c_] = __builtin_elementwise_fma(f32x2{(P1), (P1)}, vf_, acc[1][c_]); } } while (0)
#pragma unroll
    for (int i = 0; i < 16; i += 4) {
      gate(DUE(80 + i / 2));
      A[i / 2] = ldnt(vb + SOFF(16 + i / 2) + lane16); A[i / 2 + 1] = ldnt(vb + SOFF(17 + i / 2) + lane16);
      SB();
      const f32x4 pa = *(const f32x4*)(pl + i * 2), pb = *(const f32x4*)(pl + i * 2 + 4);
      PV(B[i], pa[0], pa[1]); PV(B[i + 1], pa[2], pa[3]); PV(B[i + 2], pb[0], pb[1]); PV(B[i + 3], pb[2], pb[3]);
      SB();
    }
    BAR();
#pragma unroll 1
    for (int it = 2; it < 7; ++it) {
#pragma unroll
      for (int i = 0; i < 8; i += 2) {
        const f32x4 pa = *(const f32x4*)(pl + (it >> 1) * 64 + (it & 1) * 16 + i * 2);
        PV(A[i], pa[0], pa[1]); PV(A[i + 1], pa[2], pa[3]);
        const int n = it * 8 + i + 8;
        SB();
        gate(DUE(64 + n));
        A[i] = ldnt(vb + SOFF(n) + lane16); A[i + 1] = ldnt(vb + SOFF(n + 1) + lane16);
        SB();
      }
    }
    STAMP(14);
#pragma unroll
    for (int i = 0; i < 8; i += 2) {
      const f32x4 pa = *(const f32x4*)(pl + 3 * 64 + 16 + i * 2);
      PV(A[i], pa[0], pa[1]); PV(A[i + 1], pa[2], pa[3]);
    }
#pragma unroll
    for (int c = 0; c < 4; ++c) asm volatile("" : "+v"(acc[0][c]), "+v"(acc[1][c]));
    SB();
    {
      const int wv = wave < 7 ? wave : 0;
      const gptr wp = (gptr)(br ? wo1 : wo0) + (kvo * 512 + 32 * j) * (DM * 2) + wv * 128;
#pragma unroll
      for (int i = 0; i < 4; ++i) wov[i] = ldnt(wp + (unsigned)((4 * (lane >> 3) + i) * (DM * 2) + (lane & 7) * 16));
      SB();
    }
    STAMP(6);
  } else {
    const __amdgpu_buffer_rsrc_t rs = __builtin_amdgcn_make_buffer_rsrc((void*)wsp, 0, WS_END, 0x00020000);
    const unsigned ep = __builtin_amdgcn_raw_buffer_load_b32(rs, WS_EPOCH, 0, AUX_SC1);
    const int l = lane < 56 ? lane : 0;
    const v4u gx = *(const GAS v4u*)((gptr)xp + ((16 * rid >= DM) ? DM * 2 : 0) + l * 16), gw = *(const GAS v4u*)((gptr)gwp + l * 16);
    tag = __builtin_amdgcn_readfirstlane(ep) + 1u;
    BAR();
    if (lane < 16) {
      f32x4 q4 = *(const f32x4*)&lds[L_QP + 4 * lane];
#pragma unroll
      for (int w = 1; w < 8; ++w) q4 += *(const f32x4*)&lds[L_QP + w * 64 + 4 * lane];
      const v4u gr = {f2bf(q4[0]) | (f2bf(q4[1]) << 16), tag, f2bf(q4[2]) | (f2bf(q4[3]) << 16), tag};
      __builtin_amdgcn_raw_buffer_store_b128(gr, rs, WS_QG + ((br * 8 + kvo) * 2 + (lane >> 3)) * 2048 + j * 128 + (lane & 7) * 16, 0, AUX_SC1);
    }
    STAMP(1);
    {
      float z = 0.f;
#pragma unroll
      for (int c = 0; c < 4; ++c) z += rbf(bf_lo(gx[c]) * bf_lo(gw[c])) + rbf(bf_hi(gx[c]) * bf_hi(gw[c]));
      if (lane >= 56) z = 0.f;
      z += dpp<XOR1>(z); z += dpp<XOR2>(z); z += dpp<ROR4>(z); z += dpp<ROR8>(z);
      z = tr16(z, z); z = tr32(z, z);
      z = rbf(z);
      if (lane == 0) { lds[L_G] = rbf(1.f / (1.f + __expf(-z))); lds[L_G + 1] = u2f(tag); }
    }
    const int qoff = WS_QG + ((br * 8 + kvh) * 2 + b) * 2048 + lane * 32;
    v4u qa, qb;
    for (unsigned spins = 0;; ++spins) {
      __builtin_amdgcn_s_sleep(8);
      asm volatile("" ::: "memory");
      qa = __builtin_amdgcn_raw_buffer_load_b128(rs, qoff, 0, AUX_SC1);
      qb = __builtin_amdgcn_raw_buffer_load_b128(rs, qoff + 16, 0, AUX_SC1);
      if (__all(qa[1] == tag && qa[3] == tag && qb[1] == tag && qb[3] == tag) || spins >= SPIN_CAP) break;
    }
    *(v4u*)&lds[L_Q + lane * 4] = v4u{qa[0], qa[2], qb[0], qb[2]};
    STAMP(3);
    BAR();
    BAR();
    BAR();
    if (lane < 8) {
      const int h = lane & 1;
      const f32x4 ma = *(const f32x4*)&lds[L_M + h * 8], mb = *(const f32x4*)&lds[L_M + h * 8 + 4];
      const f32x4 la = *(const f32x4*)&lds[L_L + h * 8], lb = *(const f32x4*)&lds[L_L + h * 8 + 4];
      const float Mh = fmaxf(fmaxf(fmaxf(ma[0], ma[1]), fmaxf(ma[2], ma[3])), fmaxf(fmaxf(mb[0], mb[1]), fmaxf(mb[2], mb[3]))) * C2;
      const float L = ((la[0] + la[1]) + (la[2] + la[3])) + ((lb[0] + lb[1]) + (lb[2] + lb[3]));
      __builtin_amdgcn_raw_buffer_store_b128(v4u{f2u(Mh), tag, f2u(L), tag}, rs, WS_P1 + wg * P1_STRIDE + 4096 + lane * 16, 0, AUX_SC1);
    }
    {
      const int soff = WS_P1 + (((br * 2 + ((lane >> 3) & 1)) * 8 + (lane & 7)) * 8 + kvo) * P1_STRIDE + 4096 + (j >> 3) * 16;
      v4u gs;
      for (unsigned spins = 0;; ++spins) {
        __builtin_amdgcn_s_sleep(32);
        asm volatile("" ::: "memory");
        gs = __builtin_amdgcn_raw_buffer_load_b128(rs, soff, 0, AUX_SC1);
        if (__all(gs[1] == tag && gs[3] == tag) || spins >= SPIN_CAP) break;
      }
      const float ms = u2f(gs[0]);
      float M = fmaxf(ms, dpp<XOR1>(ms)); M = fmaxf(M, dpp<XOR2>(M)); M = fmaxf(M, dpp<XOR7>(M));
      const float f = __builtin_amdgcn_exp2f(ms - M);
      float L = f * u2f(gs[2]); L += dpp<XOR1>(L); L += dpp<XOR2>(L); L += dpp<XOR7>(L);
      if (lane < 16) lds[L_W + lane] = f / L;
    }
    STAMP(5);
#pragma unroll
    for (int c = 0; c < 4; ++c) { acc[0][c] = acc[1][c] = f32x2{0.f, 0.f}; wov[c] = v4u{0u, 0u, 0u, 0u}; }
    asm volatile("s_waitcnt lgkmcnt(0)" ::: "memory");
    return;
  }

  const __amdgpu_buffer_rsrc_t rt = __builtin_amdgcn_make_buffer_rsrc((void*)wsp, 0, helper ? 0 : WS_END, 0x00020000);
  const __amdgpu_buffer_rsrc_t ro = __builtin_amdgcn_make_buffer_rsrc((void*)outp, 0, helper ? 0 : 2 * DM * 2, 0x00020000);
  const unsigned cap = helper ? 1u : SPIN_CAP;
  const int te = helper ? lane : tid;
  {
    f32x4 o0, o1;
    o0[0] = tr32(acc[0][0][0], acc[1][0][0]); o0[1] = tr32(acc[0][0][1], acc[1][0][1]);
    o0[2] = tr32(acc[0][1][0], acc[1][1][0]); o0[3] = tr32(acc[0][1][1], acc[1][1][1]);
    o1[0] = tr32(acc[0][2][0], acc[1][2][0]); o1[1] = tr32(acc[0][2][1], acc[1][2][1]);
    o1[2] = tr32(acc[0][3][0], acc[1][3][0]); o1[3] = tr32(acc[0][3][1], acc[1][3][1]);
    *(f32x4*)&lds[L_ACC + wave * 512 + lane * 8] = o0;
    *(f32x4*)&lds[L_ACC + wave * 512 + lane * 8 + 4] = o1;
  }
  if (!helper) BAR();
  if ((wave & 7) < 4) {
    f32x2 o = *(const f32x2*)&lds[L_ACC + 2 * te];
#pragma unroll
    for (int w = 1; w < 8; ++w) o += *(const f32x2*)&lds[L_ACC + w * 512 + 2 * te];
    __builtin_amdgcn_raw_buffer_store_b128(v4u{f2u(o[0]), tag, f2u(o[1]), tag}, rt, WS_P1 + wg * P1_STRIDE + te * 16, 0, AUX_SC1);
  }
  STAMP(7);
  if (wave >= 6) {
    const int rg = lane >> 3;
    const int doff = WS_P1 + (((br * 2 + (wave & 1)) * 8 + (lane & 7)) * 8 + kvo) * P1_STRIDE + (32 * j + 4 * rg) * 8;
    const float wt = lds[L_W + (wave & 1) * 8 + (lane & 7)];
    v4u ga = {0u, ~tag, 0u, ~tag}, gb = {0u, ~tag, 0u, ~tag};
    for (unsigned spins = 0;; ++spins) {
      asm volatile("" ::: "memory");
      const bool need = !(ga[1] == tag && ga[3] == tag && gb[1] == tag && gb[3] == tag);
      if (!__any(need) || spins > cap) break;
      if (spins) __builtin_amdgcn_s_sleep(SLEEP1);
      if (need) {
        ga = __builtin_amdgcn_raw_buffer_load_b128(rt, doff, 0, AUX_SC1);
        gb = __builtin_amdgcn_raw_buffer_load_b128(rt, doff + 16, 0, AUX_SC1);
      }
    }
    float o[4] = {wt * u2f(ga[0]), wt * u2f(ga[2]), wt * u2f(gb[0]), wt * u2f(gb[2])};
#pragma unroll
    for (int i = 0; i < 4; ++i) { o[i] += dpp<XOR1>(o[i]); o[i] += dpp<XOR2>(o[i]); o[i] += dpp<XOR7>(o[i]); o[i] = rbf(o[i]); }
    if ((lane & 7) == 0) *(f32x4*)&lds[L_O + (wave - 6) * 32 + 4 * rg] = f32x4{o[0], o[1], o[2], o[3]};
  }
  STAMP(11);
  f32x2 wf[4][4];
#pragma unroll
  for (int i = 0; i < 4; ++i)
#pragma unroll
    for (int c = 0; c < 4; ++c) { wf[i][c] = f32x2{bf_lo(wov[i][c]), bf_hi(wov[i][c])}; asm volatile("" : "+v"(wf[i][c])); }
  STAMP(12);
  if (!helper) BAR();
  STAMP(8);
  if (wave != 7) {
    const f32x4 oa = *(const f32x4*)&lds[L_O + 4 * (lane >> 3)], ob = *(const f32x4*)&lds[L_O + 32 + 4 * (lane >> 3)];
    f32x2 a[2][4];
#pragma unroll
    for (int c = 0; c < 4; ++c) { a[0][c] = f32x2{oa[0], oa[0]} * wf[0][c]; a[1][c] = f32x2{ob[0], ob[0]} * wf[0][c]; }
#pragma unroll
    for (int i = 1; i < 4; ++i)
#pragma unroll
      for (int c = 0; c < 4; ++c) {
        a[0][c] = __builtin_elementwise_fma(f32x2{oa[i], oa[i]}, wf[i][c], a[0][c]);
        a[1][c] = __builtin_elementwise_fma(f32x2{ob[i], ob[i]}, wf[i][c], a[1][c]);
      }
    float y[8], z[4];
#pragma unroll
    for (int k = 0; k < 8; ++k) { const float e0 = a[0][k >> 1][k & 1], e1 = a[1][k >> 1][k & 1]; y[k] = tr32(e0, e1); }
#pragma unroll
    for (int k = 0; k < 4; ++k) z[k] = tr16(y[k], y[k + 4]);
    const float u0 = trd<ROR8>(z[0], z[2], lane & 8), u1 = trd<ROR8>(z[1], z[3], lane & 8);
    const int oi = (lane >> 5) * DM + 64 * (wave & 7) + 8 * (lane & 7) + ((lane >> 4) & 1) * 4 + ((lane >> 3) & 1) * 2;
    __builtin_amdgcn_raw_buffer_store_b128(v4u{f2u(u0), tag, f2u(u1), tag}, rt, WS_P2 + wg * P2_STRIDE + oi * 8, 0, AUX_SC1);
  }
  STAMP(9);
  if ((wg >> 3) < 7) {
    const int off = WS_P2 + (te >> 3) * P2_STRIDE + rid * 128 + (te & 7) * 16;
    v4u gy[4];
#pragma unroll
    for (int i = 0; i < 4; ++i) gy[i] = v4u{0u, ~tag, 0u, ~tag};
    for (unsigned spins = 0;; ++spins) {
      asm volatile("" ::: "memory");
      bool need[4];
#pragma unroll
      for (int i = 0; i < 4; ++i) need[i] = !(gy[i][1] == tag && gy[i][3] == tag);
      if (!__any(need[0] || need[1] || need[2] || need[3]) || spins > cap) break;
      if (spins) __builtin_amdgcn_s_sleep(SLEEP2);
#pragma unroll
      for (int i = 0; i < 4; ++i) if (need[i]) gy[i] = __builtin_amdgcn_raw_buffer_load_b128(rt, off + i * 64 * P2_STRIDE, 0, AUX_SC1);
    }
    STAMP(13);
    const float a0 = u2f(gy[0][0]) + u2f(gy[1][0]), a1 = u2f(gy[0][2]) + u2f(gy[1][2]);
    const float b0 = u2f(gy[2][0]) + u2f(gy[3][0]), b1 = u2f(gy[2][2]) + u2f(gy[3][2]);
    float y = tr16(tr32(a0, b0), tr32(a1, b1));
    y += dpp<ROR8>(y);
    if (!(lane & 8)) lds[L_RED + wave * 32 + (lane >> 5) * 16 + (lane & 7) * 2 + ((lane >> 4) & 1)] = y;
    if (!helper) BAR();
    if (te < 32) {
      float t = lds[L_RED + te];
#pragma unroll
      for (int w = 1; w < 8; ++w) t += lds[L_RED + w * 32 + te];
      const float gt = lds[L_G];
      t = rbf(((te & 16) ? rbf(1.f - gt) : gt) * rbf(t));
      const float r = tr16(t, t);
      if (te < 16) __builtin_amdgcn_raw_buffer_store_b16((unsigned short)f2bf(r), ro, (rid * 16 + te) * 2, 0, 0);
      if (wg == 0 && te == 0) __builtin_amdgcn_raw_buffer_store_b32(tag, rt, WS_EPOCH, 0, AUX_SC1);
    }
  }
  STAMP(10);
}

extern "C" void kernel_launch(void* const* d_in, const int* in_sizes, int n_in, void* d_out, int out_size, void* d_ws, size_t ws_size,
                              hipStream_t stream) {
  Params p;
  p.x = (const uint16_t*)d_in[0];
  p.k[0] = (const uint16_t*)d_in[1]; p.v[0] = (const uint16_t*)d_in[2]; p.wq[0] = (const uint16_t*)d_in[3]; p.wo[0] = (const uint16_t*)d_in[4];
  p.k[1] = (const uint16_t*)d_in[6]; p.v[1] = (const uint16_t*)d_in[7]; p.wq[1] = (const uint16_t*)d_in[8]; p.wo[1] = (const uint16_t*)d_in[9];
  p.gw = (const uint16_t*)d_in[10];
  p.out = (uint16_t*)d_out;
  p.ws = (unsigned char*)d_ws;
  hipLaunchKernelGGL(gqa2_fused, dim3(NWG), dim3(NTHR), 0, stream, p);
}
```

```cpp
#include <hip/hip_runtime.h>
#include <hip/hip_bf16.h>
#include <stdint.h>

typedef unsigned v4u __attribute__((ext_vector_type(4)));
typedef unsigned v2u __attribute__((ext_vector_type(2)));
typedef float f32x2 __attribute__((ext_vector_type(2)));
typedef float f32x4 __attribute__((ext_vector_type(4)));
typedef __bf16 bf16x2 __attribute__((ext_vector_type(2)));
typedef __bf16 bf16x8 __attribute__((ext_vector_type(8)));
typedef short s16x4 __attribute__((ext_vector_type(4)));
typedef unsigned long long u64x2 __attribute__((ext_vector_type(2)));
typedef unsigned long long u64x4 __attribute__((ext_vector_type(4)));
#define GAS __attribute__((address_space(1)))
typedef const GAS unsigned char* gptr;

#define T_STREAM 35.1
constexpr unsigned TB_OFF = 160;
constexpr unsigned QPOLL_AT = 600;
constexpr float QD0 = 305.f;
constexpr unsigned WO_DELAY = 30;
constexpr unsigned T1ABS = 208, T2ABS = 324;
constexpr int INFL = 6;
constexpr int ROT_KB = 264;
constexpr int WO_N1 = 12;
constexpr int CODE_BYTES = 21504;

constexpr int DM = 448, NQ = 4096;
constexpr int NWG = 256, NTHR = 768;
constexpr unsigned TAU16 = (unsigned)(T_STREAM * 1600.0 / 128.0 + 0.5);
constexpr unsigned SPIN_CAP = 1u << 19;

constexpr int WS_EPOCH = 0;
constexpr int WS_QG = 4096;
constexpr int WS_TS = WS_QG + 32 * 2048;
constexpr int P1_STRIDE = 4096 + 128;
constexpr int WS_P1 = WS_TS + NWG * 128;
constexpr int P2_STRIDE = 896 * 8 + 128;
constexpr int WS_P2 = WS_P1 + 2 * NWG * P1_STRIDE;
constexpr int WS_END = WS_P2 + NWG * P2_STRIDE;

constexpr int L_P = 0;
constexpr int L_ACC = L_P + 8 * 256;
constexpr int L_QP = L_ACC + 16 * 512;
constexpr int L_Q = L_QP + 4 * 64;
constexpr int L_O = L_Q + 512;
constexpr int L_RED = L_O + 32;
constexpr int L_M = L_RED + 16 * 8;
constexpr int L_L = L_M + 32;
constexpr int L_G = L_L + 32;
constexpr int L_F = L_G + 4;
constexpr int F_QP = L_F, F_Q = L_F + 1, F_M = L_F + 2, F_L = L_F + 4, F_V = L_F + 5, F_A = L_F + 6, F_WO = L_F + 7;
constexpr int L_WO = L_F + 8;
constexpr int L_TOTAL = L_WO + 32 * DM / 2;

struct Params {
  const uint16_t *k[2], *v[2];
  const uint16_t* w[2][2];
  const uint16_t* x;
  unsigned char* ws;
  const uint16_t* gw;
  uint16_t* out;
};

__device__ __forceinline__ float u2f(unsigned u) { return __builtin_bit_cast(float, u); }
__device__ __forceinline__ unsigned f2u(float f) { return __builtin_bit_cast(unsigned, f); }
__device__ __forceinline__ float dot2(unsigned k, unsigned q, float acc) {
  return __builtin_amdgcn_fdot2_f32_bf16(__builtin_bit_cast(bf16x2, k), __builtin_bit_cast(bf16x2, q), acc, false);
}
__device__ __forceinline__ float bf_lo(unsigned u) { return u2f(u << 16); }
__device__ __forceinline__ float bf_hi(unsigned u) { return u2f(u & 0xffff0000u); }
__device__ __forceinline__ unsigned f2bf(float f) { return __builtin_bit_cast(unsigned short, (__bf16)f); }
__device__ __forceinline__ float rbf(float f) { return (float)(__bf16)f; }
template <int CTRL> __device__ __forceinline__ float dpp(float x) {
  return u2f(__builtin_amdgcn_mov_dpp(__builtin_bit_cast(int, x), CTRL, 0xf, 0xf, true));
}
constexpr int XOR1 = 0xB1, XOR2 = 0x4E, XOR7 = 0x141, ROR8 = 0x128, ROR4 = 0x124, ROR12 = 0x12C;
__device__ __forceinline__ float tr16(float a, float b) {
  auto r = __builtin_amdgcn_permlane16_swap(f2u(a), f2u(b), false, false);
  return u2f(r[0]) + u2f(r[1]);
}
__device__ __forceinline__ float tr32(float a, float b) {
  auto r = __builtin_amdgcn_permlane32_swap(f2u(a), f2u(b), false, false);
  return u2f(r[0]) + u2f(r[1]);
}
template <int CTRL> __device__ __forceinline__ float trd(float a, float b, bool bit) {
  const float t1 = a + dpp<CTRL>(a), t2 = b + dpp<CTRL>(b);
  return bit ? t2 : t1;
}
__device__ __forceinline__ float max16(float x) {
  auto r = __builtin_amdgcn_permlane16_swap(f2u(x), f2u(x), false, false);
  return fmaxf(u2f(r[0]), u2f(r[1]));
}
__device__ __forceinline__ float max32(float x) {
  auto r = __builtin_amdgcn_permlane32_swap(f2u(x), f2u(x), false, false);
  return fmaxf(u2f(r[0]), u2f(r[1]));
}
__device__ __forceinline__ v4u ldnt(gptr p) { return __builtin_nontemporal_load((const GAS v4u*)p); }
#define SB() __builtin_amdgcn_sched_barrier(0)
constexpr int AUX_SC1 = 16;
__device__ __forceinline__ void gate(unsigned due) {
  asm volatile("s_waitcnt vmcnt(%1)\n"
               "1:\n\ts_memrealtime vcc\n\ts_waitcnt lgkmcnt(0)\n\ts_sub_u32 vcc_lo, vcc_lo, %0\n\ts_cmp_lt_i32 vcc_lo, 0\n\t"
               "s_cbranch_scc0 2f\n\ts_sleep 1\n\ts_branch 1b\n2:" ::"s"(due), "i"(INFL - 2) : "vcc", "scc", "memory");
}

#define LAS __attribute__((address_space(3)))
__device__ __forceinline__ void lds_signal(float* w, unsigned v, int lane) {
  asm volatile("s_waitcnt lgkmcnt(0)" ::: "memory");
  if (lane == 0) (void)__hip_atomic_fetch_add((LAS unsigned*)w, v, __ATOMIC_RELAXED, __HIP_MEMORY_SCOPE_WORKGROUP);
  asm volatile("" ::: "memory");
}
__device__ __forceinline__ unsigned lds_wait(float* w, unsigned n) {
  unsigned v;
  for (unsigned spins = 0;; ++spins) {
    v = __builtin_amdgcn_readfirstlane(__hip_atomic_load((LAS unsigned*)w, __ATOMIC_RELAXED, __HIP_MEMORY_SCOPE_WORKGROUP));
    if (v >= n || spins > (SPIN_CAP << 5)) break;
    __builtin_amdgcn_s_sleep(1);
  }
  asm volatile("" ::: "memory");
  return v;
}
__device__ __forceinline__ void wait_until(unsigned due) {
  asm volatile("1:\n\ts_memrealtime vcc\n\ts_waitcnt lgkmcnt(0)\n\ts_sub_u32 vcc_lo, vcc_lo, %0\n\ts_cmp_lt_i32 vcc_lo, 0\n\t"
               "s_cbranch_scc0 2f\n\ts_sleep 1\n\ts_branch 1b\n2:" ::"s"(due) : "vcc", "scc", "memory");
}
__device__ __forceinline__ unsigned now() { return (unsigned)__builtin_amdgcn_s_memrealtime(); }
struct Fold { float y[2], z[2], u[2]; };
#define DOT4(KV, Q) dot2((KV)[3], (Q)[3], dot2((KV)[2], (Q)[2], dot2((KV)[1], (Q)[1], dot2((KV)[0], (Q)[0], 0.f))))
template <int P> __device__ __forceinline__ float fold_pair(Fold& f, const v4u& ka, const v4u& kb, const v4u& q0, const v4u& q1, int lane) {
  float e0 = tr16(DOT4(ka, q0), DOT4(kb, q0)), e1 = tr16(DOT4(ka, q1), DOT4(kb, q1));
  if (!(P & 1)) { f.y[0] = e0; f.y[1] = e1; return 0.f; }
  e0 = trd<ROR8>(f.y[0], e0, lane & 8); e1 = trd<ROR8>(f.y[1], e1, lane & 8);
  if (!(P & 2)) { f.z[0] = e0; f.z[1] = e1; return 0.f; }
  e0 = trd<XOR7>(f.z[0], e0, lane & 4); e1 = trd<XOR7>(f.z[1], e1, lane & 4);
  if (!(P & 4)) { f.u[0] = e0; f.u[1] = e1; return 0.f; }
  e0 = trd<XOR2>(f.u[0], e0, lane & 2); e1 = trd<XOR2>(f.u[1], e1, lane & 2);
  return rbf(trd<XOR1>(e0, e1, lane & 1));
}

#define BAR() asm volatile("s_waitcnt lgkmcnt(0)\n\ts_barrier" ::: "memory")
constexpr float C2 = 0.0625f * 1.4426950408889634f;

__global__ void __launch_bounds__(NTHR, 3) gqa2_fused(Params p) {
  __shared__ __attribute__((aligned(16))) float lds[L_TOTAL];
  const int wg = blockIdx.x, lane = threadIdx.x & 63, wv12 = __builtin_amdgcn_readfirstlane(threadIdx.x >> 6);
  const int b = wg >> 7, sp = (wg >> 3) & 15, kvh = wg & 7;
  const int pr = b, j = sp, kvo = (kvh + 1) & 7;
  u64x4 kaa, kac;
  u64x2 kab;
  unsigned long long t64;
  asm volatile("s_memrealtime %3\n\ts_load_dwordx8 %0, %4, 0x0\n\ts_load_dwordx4 %1, %4, %5\n\ts_load_dwordx8 %2, %4, 0x40"
               : "=&s"(kaa), "=&s"(kab), "=&s"(kac), "=&s"(t64) : "s"(__builtin_amdgcn_kernarg_segment_ptr()), "s"(32 + pr * 16));
  const unsigned lane16 = lane * 16u;
  const __amdgpu_buffer_rsrc_t rc = __builtin_amdgcn_make_buffer_rsrc((void*)(__builtin_amdgcn_s_getpc() & ~255ull), 0, wv12 == 0 ? CODE_BYTES : 0, 0x00020000);
  v4u cpf = __builtin_amdgcn_raw_buffer_load_b128(rc, lane16 + (wg >> 3) * 1024, 0, 0);
  const bool helper = wv12 < 4;
  const int wave = wv12 - 4;
  const unsigned reg = (unsigned)(b * 8 + kvh) << 22;
  const unsigned kb0 = (unsigned)((sp * 8 + wave) * 2 + (b * 8 + kvh) * ROT_KB);
#define SOFF(n) (reg + (((((kb0 + (((n) & 31) >> 1) * 256u) & 4095u) + ((n) & 1))) << 10))
  const int row0 = 112 * wv12 + (lane < 56 ? 8 * (lane >> 2) : 0);
  const int col0 = kvo * 512 + 32 * j + (lane & 3) * 8;
  const unsigned hm = helper ? ~0u : 0u;
  const unsigned rsw = (unsigned)(j & 1) * (4 * NQ * 2);
  unsigned fo = (((unsigned)(row0 * (NQ * 2) + col0 * 2) + rsw) & hm) | ((SOFF(0) + lane16) & ~hm);
  unsigned fo1 = fo + 1024u + (hm & (NQ * 2 - 1024u)), xo = (unsigned)row0 * 2u;
  unsigned po = (unsigned)((j * 28 + (lane < 28 ? lane : 0)) * (NQ * 2) + kvo * 1024 + 384);
  int wsel = wv12;
  asm volatile("" : "+v"(fo), "+v"(fo1), "+v"(xo), "+v"(po));
  asm volatile("s_waitcnt lgkmcnt(0)" : "+s"(wsel), "+s"(kaa), "+s"(kab), "+s"(kac), "+s"(t64));
  const unsigned t_entry = (unsigned)t64;
  const gptr ka = (gptr)kaa[0], kbb = (gptr)kaa[1], va = (gptr)kaa[2], vbb = (gptr)kaa[3], wqp = (gptr)kab[0], wop = (gptr)kab[1];
  const gptr xp = (gptr)kac[0], gwp = (gptr)kac[2];
  GAS unsigned char* wsp = (GAS unsigned char*)kac[1];
  GAS uint16_t* outp = (GAS uint16_t*)kac[3];
  v4u A[16], xv[2];
  unsigned ep = 0, wpf;
  {
    const gptr fb = wsel < 4 ? wqp : ka;
    wpf = __builtin_amdgcn_raw_buffer_load_b32(__builtin_amdgcn_make_buffer_rsrc((void*)(size_t)wqp, 0, wsel == 0 ? DM * NQ * 2 : 0, 0x00020000), po, 0, 2);
    A[0] = ldnt(fb + fo); A[1] = ldnt(fb + fo1);
    if (wsel < 4) {
#pragma unroll
      for (int i = 2; i < 8; ++i) A[i] = ldnt(fb + (fo + (unsigned)(i * NQ * 2) - (i < 4 ? 0u : 2u * rsw)));
      xv[0] = *(const GAS v4u*)(xp + xo);
      xv[1] = *(const GAS v4u*)(xp + DM * 2 + xo);
      if (wsel == 0) ep = __hip_atomic_load((GAS unsigned*)(wsp + WS_EPOCH), __ATOMIC_RELAXED, __HIP_MEMORY_SCOPE_AGENT);
#pragma unroll
      for (int i = 0; i < WO_N1 / 4; ++i)
        __builtin_amdgcn_global_load_lds((const GAS void*)(wop + (kvo * 512 + 32 * j) * (DM * 2) + (wsel * (WO_N1 / 4) + i) * 1024 + lane16), (LAS void*)&lds[L_WO + (wsel * (WO_N1 / 4) + i) * 256], 16, 0, 2);
    }
    SB();
  }
  if (threadIdx.x < 8) lds[L_F + threadIdx.x] = 0.f;
  BAR();
  const int e1 = kvh * 32 + (wg >> 3);
  const int rid = 14 * kvh + (wg >> 3);
  const __amdgpu_buffer_rsrc_t rs = __builtin_amdgcn_make_buffer_rsrc((void*)(size_t)wsp, 0, WS_END, 0x00020000);

  int role = (wv12 - 4) >> 31;
  asm volatile("" : "+s"(role));
  if (role != 0) {
    if (wv12 == 3) {
      gptr a = ((sp & 4) ? ((sp & 2) ? vbb : va) : ((sp & 2) ? kbb : ka)) + reg + ((unsigned)(sp & 1) << 21) + 4096u * wg;
      if (sp & 8) a = (sp & 4) ? (gptr)wsp + WS_END + (wg & 31) * 128 : wop + (kvo * 512 + (sp & 1) * 511) * (DM * 2) + ((sp & 2) << 6);
      unsigned tv;
      asm volatile("s_load_dword %0, %1, 0x0\n\ts_waitcnt lgkmcnt(0)" : "=s"(tv) : "s"(a) : "memory");
    }
    {
      if (rsw) { xv[0] = v4u{xv[0][2], xv[0][3], xv[0][0], xv[0][1]}; xv[1] = v4u{xv[1][2], xv[1][3], xv[1][0], xv[1][1]}; }
      f32x2 a[2][4];
#pragma unroll
      for (int c = 0; c < 4; ++c) a[0][c] = a[1][c] = f32x2{0.f, 0.f};
#pragma unroll
      for (int i = 0; i < 8; ++i) {
        const float x0 = (i & 1) ? bf_hi(xv[0][i >> 1]) : bf_lo(xv[0][i >> 1]);
        const float x1 = (i & 1) ? bf_hi(xv[1][i >> 1]) : bf_lo(xv[1][i >> 1]);
#pragma unroll
        for (int c = 0; c < 4; ++c) {
          const f32x2 wf = {bf_lo(A[i][c]), bf_hi(A[i][c])};
          a[0][c] = __builtin_elementwise_fma(f32x2{x0, x0}, wf, a[0][c]); a[1][c] = __builtin_elementwise_fma(f32x2{x1, x1}, wf, a[1][c]);
        }
      }
      float y[8], z[4], u[2];
#pragma unroll
      for (int k = 0; k < 8; ++k) {
        const float e0 = a[0][k >> 1][k & 1], e1 = a[1][k >> 1][k & 1];
        y[k] = tr32(lane < 56 ? e0 : 0.f, lane < 56 ? e1 : 0.f);
      }
#pragma unroll
      for (int k = 0; k < 4; ++k) z[k] = tr16(y[k], y[k + 4]);
#pragma unroll
      for (int k = 0; k < 2; ++k) u[k] = trd<ROR8>(z[k], z[k + 2], lane & 8);
      const float keep = (lane & 4) ? u[1] : u[0], send = (lane & 4) ? u[0] : u[1];
      const float o4a = dpp<ROR4>(send), o4b = dpp<ROR12>(send);
      lds[L_QP + wv12 * 64 + (lane >> 5) * 32 + (lane & 3) * 8 + ((lane >> 2) & 7)] = keep + ((lane & 4) ? o4a : o4b);
      lds_signal(&lds[F_QP], 1u, lane);
    }
    if (wv12 == 1 || wv12 == 2) {
      const int l = lane < 56 ? lane : 0;
      const v4u gx = *(const GAS v4u*)(xp + (wv12 - 1) * (DM * 2) + l * 16), gw = *(const GAS v4u*)(gwp + l * 16);
      float z = 0.f;
#pragma unroll
      for (int c = 0; c < 4; ++c) z += rbf(bf_lo(gx[c]) * bf_lo(gw[c])) + rbf(bf_hi(gx[c]) * bf_hi(gw[c]));
      if (lane >= 56) z = 0.f;
      z += dpp<XOR1>(z); z += dpp<XOR2>(z); z += dpp<ROR4>(z); z += dpp<ROR8>(z);
      z = tr16(z, z); z = tr32(z, z);
      z = rbf(z);
      if (lane == 0) lds[L_G + wv12 - 1] = rbf(1.f / (1.f + __expf(-z)));
    } else if (wv12 == 0) {
      asm volatile("" ::"v"(cpf), "v"(wpf));
      const unsigned tag = __builtin_amdgcn_readfirstlane(ep) * 1664525u + 1013904223u;
      lds_wait(&lds[F_QP], 4u);
      if (lane < 16) {
        f32x4 q4 = *(const f32x4*)&lds[L_QP + 4 * lane];
#pragma unroll
        for (int w = 1; w < 4; ++w) q4 += *(const f32x4*)&lds[L_QP + w * 64 + 4 * lane];
        const v4u gr = {f2bf(q4[0]) | (f2bf(q4[1]) << 16), tag, f2bf(q4[2]) | (f2bf(q4[3]) << 16), tag};
        __builtin_amdgcn_raw_buffer_store_b128(gr, rs, WS_QG + ((kvo * 2 + (lane >> 3)) * 2 + pr) * 2048 + j * 128 + (lane & 7) * 16, 0, AUX_SC1);
      }
      const unsigned qd = now() - t_entry;
      if (lane < 8) __builtin_amdgcn_raw_buffer_store_b128(v4u{t_entry, tag, qd, tag}, rs, WS_TS + wg * 128 + lane16, 0, AUX_SC1);
      const int qoff = WS_QG + (kvh * 2 + b) * 4096 + lane * 32;
      v4u qg[4];
      wait_until(t_entry + QPOLL_AT);
      const v4u ts = __builtin_amdgcn_raw_buffer_load_b128(rs, WS_TS + ((lane & 31) * 8 + ((lane + kvh) & 7)) * 128, 0, AUX_SC1);
      for (unsigned spins = 0;; ++spins) {
        if (spins) __builtin_amdgcn_s_sleep(8);
        asm volatile("" ::: "memory");
        bool ok = true;
#pragma unroll
        for (int i = 0; i < 4; ++i) qg[i] = __builtin_amdgcn_raw_buffer_load_b128(rs, qoff + (i >> 1) * 2048 + (i & 1) * 16, 0, AUX_SC1);
#pragma unroll
        for (int i = 0; i < 4; ++i) ok = ok && qg[i][1] == tag && qg[i][3] == tag;
        if (__all(ok) || spins >= SPIN_CAP) break;
      }
      *(v4u*)&lds[L_Q + lane * 4] = v4u{qg[0][0], qg[0][2], qg[1][0], qg[1][2]};
      *(v4u*)&lds[L_Q + 256 + lane * 4] = v4u{qg[2][0], qg[2][2], qg[3][0], qg[3][2]};
      const bool tin = ts[1] == tag && ts[3] == tag;
      float dx = tin ? fminf((float)(int)(ts[0] - t_entry), 500.f) : 0.f, dq = tin ? fminf((float)ts[2], 700.f) : (float)QPOLL_AT;
      float dm = dx;
      dm += dpp<XOR1>(dm); dm += dpp<XOR2>(dm); dm += dpp<XOR7>(dm); dm += dpp<ROR8>(dm); dm = tr16(dm, dm) * (1.f / 32.f);
      dx = fmaxf(dx, dpp<XOR1>(dx)); dx = fmaxf(dx, dpp<XOR2>(dx)); dx = fmaxf(dx, dpp<XOR7>(dx)); dx = fmaxf(dx, dpp<ROR8>(dx)); dx = max16(dx);
      dq += dpp<XOR1>(dq); dq += dpp<XOR2>(dq); dq += dpp<XOR7>(dq); dq += dpp<ROR8>(dq); dq = tr16(dq, dq) * (1.f / 32.f) - QD0;
      dq = fmaxf(fmaxf(dq - 0.8f * (dx - dm), 0.25f * dq), 0.f);
      const unsigned t_org = __builtin_amdgcn_readfirstlane(t_entry + (unsigned)(int)(fmaxf(dx, -500.f) + dq));
      if (lane == 0) { lds[L_G + 2] = u2f(tag); lds[L_G + 3] = u2f(t_org); }
      lds_signal(&lds[F_Q], 1u, lane);
      wait_until(t_org + 2600);
      lds_wait(&lds[F_L], 16u);
      if (lane < 16) {
        const int e = (lane >> 3) * 2 + (lane & 1);
        const f32x4 ma = *(const f32x4*)&lds[L_M + e * 8], mb = *(const f32x4*)&lds[L_M + e * 8 + 4];
        const f32x4 la = *(const f32x4*)&lds[L_L + e * 8], lb = *(const f32x4*)&lds[L_L + e * 8 + 4];
        const float Mh = fmaxf(fmaxf(fmaxf(ma[0], ma[1]), fmaxf(ma[2], ma[3])), fmaxf(fmaxf(mb[0], mb[1]), fmaxf(mb[2], mb[3]))) * C2;
        const float L = ((la[0] + la[1]) + (la[2] + la[3])) + ((lb[0] + lb[1]) + (lb[2] + lb[3]));
        __builtin_amdgcn_raw_buffer_store_b128(v4u{f2u(Mh), tag, f2u(L), tag}, rs, WS_P1 + ((lane >> 3) * NWG + e1) * P1_STRIDE + 4096 + (lane & 7) * 16, 0, AUX_SC1);
      }
      wait_until(t_org + TB_OFF + ((126u * TAU16) >> 4));
      lds_wait(&lds[F_A], 8u);
#pragma unroll
      for (int i = 0; i < 4; ++i) {
        f32x2 o = *(const f32x2*)&lds[L_ACC + i * 128 + 2 * lane];
#pragma unroll
        for (int w = 1; w < 8; ++w) o += *(const f32x2*)&lds[L_ACC + w * 512 + i * 128 + 2 * lane];
        __builtin_amdgcn_raw_buffer_store_b128(v4u{f2u(o[0]), tag, f2u(o[1]), tag}, rs, WS_P1 + e1 * P1_STRIDE + i * 1024 + lane16, 0, AUX_SC1);
      }
      const gptr wp = wop + (kvo * 512 + 32 * j) * (DM * 2) + lane16;
      lds_wait(&lds[F_V], 8u);
      wait_until(now() + WO_DELAY);
#pragma unroll 4
      for (int i = WO_N1; i < 28; ++i)
        __builtin_amdgcn_global_load_lds((const GAS void*)(wp + i * 1024), (LAS void*)&lds[L_WO + i * 256], 16, 0, 2);
      BAR();
      asm volatile("s_waitcnt vmcnt(0)" ::: "memory");
      lds_signal(&lds[F_WO], 1u, lane);
    }
    asm volatile("" ::"v"(cpf), "v"(wpf));
    asm volatile("s_waitcnt vmcnt(0) lgkmcnt(0)" ::: "memory");
    return;
  }

  const int tid = wave * 64 + lane;
  v4u B[16];
  unsigned tb = t_entry + TB_OFF;
#define DUE(n) (tb + (((unsigned)(n) * TAU16) >> 4))
#pragma unroll
  for (int i = 2; i < 24; i += 2) {
    gate(DUE(i));
    if (i < 16) { A[i] = ldnt(ka + SOFF(i) + lane16); A[i + 1] = ldnt(ka + SOFF(i + 1) + lane16); }
    else { B[i - 16] = ldnt(ka + SOFF(i) + lane16); B[i - 15] = ldnt(ka + SOFF(i + 1) + lane16); }
    SB();
  }
  asm volatile("" ::"v"(cpf), "v"(wpf));
  lds_wait(&lds[F_Q], 1u);
  v4u q0 = *(const v4u*)&lds[L_Q + (lane & 31) * 4], q1 = *(const v4u*)&lds[L_Q + 128 + (lane & 31) * 4];
  const v4u qb0 = *(const v4u*)&lds[L_Q + 256 + (lane & 31) * 4], qb1 = *(const v4u*)&lds[L_Q + 384 + (lane & 31) * 4];
  const unsigned tag = __builtin_amdgcn_readfirstlane(f2u(lds[L_G + 2]));
  tb = __builtin_amdgcn_readfirstlane(f2u(lds[L_G + 3])) + TB_OFF + 5;
  float* const pw = &lds[L_P + wave * 256 + (lane & 33) + (((lane >> 4) & 1) | ((lane >> 2) & 2) | (lane & 4) | ((lane << 2) & 8)) * 2];
#define MAX_STEP(BR) do { float m = fmaxf(fmaxf(s0, s1), -3.0e38f);     \
    m = fmaxf(m, dpp<XOR2>(m)); m = fmaxf(m, dpp<ROR4>(m)); m = fmaxf(m, dpp<ROR8>(m)); m = max16(m); m = max32(m); \
    if (lane < 2) lds[L_M + ((BR) * 2 + lane) * 8 + wave] = m; \
    lds_signal(&lds[F_M + (BR)], 1u, lane); } while (0)
#define EXP_STEP(BR) do { lds_wait(&lds[F_M + (BR)], 8u); \
    const f32x4 ma = *(const f32x4*)&lds[L_M + ((BR) * 2 + (lane & 1)) * 8], mb = *(const f32x4*)&lds[L_M + ((BR) * 2 + (lane & 1)) * 8 + 4]; \
    const float M = fmaxf(fmaxf(fmaxf(ma[0], ma[1]), fmaxf(ma[2], ma[3])), fmaxf(fmaxf(mb[0], mb[1]), fmaxf(mb[2], mb[3]))) * C2; \
    const float e0 = __builtin_amdgcn_exp2f(fmaf(s0, C2, -M)), e1 = __builtin_amdgcn_exp2f(fmaf(s1, C2, -M)); \
    pw[(BR) * 128] = e0; pw[(BR) * 128 + 64] = e1; \
    float l = e0 + e1; \
    l += dpp<XOR2>(l); l += dpp<ROR4>(l); l += dpp<ROR8>(l); l = tr16(l, l); l = tr32(l, l); \
    if (lane < 2) lds[L_L + ((BR) * 2 + lane) * 8 + wave] = l; \
    lds_signal(&lds[F_L], 1u, lane); } while (0)
  float s0, s1 = 0.f;
  {
    Fold f;
#define STEP0(I) gate(DUE(24 + (I) / 2)); \
    B[8 + (I) / 2] = ldnt(ka + SOFF(24 + (I) / 2) + lane16); B[9 + (I) / 2] = ldnt(ka + SOFF(25 + (I) / 2) + lane16); SB(); \
    fold_pair<(I) / 2>(f, A[I], A[(I) + 1], q0, q1, lane); s0 = fold_pair<(I) / 2 + 1>(f, A[(I) + 2], A[(I) + 3], q0, q1, lane); SB();
    STEP0(0) STEP0(4) STEP0(8) STEP0(12)
  }
#pragma unroll 1
  for (int bt = 1; bt < 4; ++bt) {
    Fold f;
    float v;
    const gptr base = (bt == 3) ? va : kbb;
#define STEP1(I) v = fold_pair<(I) / 2>(f, B[I], B[(I) + 1], q0, q1, lane); SB(); \
    gate(DUE(bt * 16 + (I) + 16)); \
    B[I] = ldnt(base + SOFF(bt * 16 + (I) + 16) + lane16); B[(I) + 1] = ldnt(base + SOFF(bt * 16 + (I) + 17) + lane16); SB();
    STEP1(0) STEP1(2) STEP1(4) STEP1(6) STEP1(8) STEP1(10) STEP1(12) STEP1(14)
    if (bt == 2) { EXP_STEP(0); s0 = v; }
    else { s1 = v; MAX_STEP(bt >> 1); }
    q0 = qb0; q1 = qb1;
  }
  f32x2 acc[2][4];
#pragma unroll
  for (int c = 0; c < 4; ++c) acc[0][c] = acc[1][c] = f32x2{0.f, 0.f};
  const float* pl = &lds[L_P + wave * 256 + (lane >> 5) * 32];
#define PV(VV, P0, P1) do { _Pragma("unroll") for (int c_ = 0; c_ < 4; ++c_) { const f32x2 vf_ = {bf_lo((VV)[c_]), bf_hi((VV)[c_])}; \
    acc[0][c_] = __builtin_elementwise_fma(f32x2{(P0), (P0)}, vf_, acc[0][c_]); acc[1][c_] = __builtin_elementwise_fma(f32x2{(P1), (P1)}, vf_, acc[1][c_]); } } while (0)
#define PV2(R, I, N) do { const f32x4 pa_ = *(const f32x4*)(pl + ((N) >> 4) * 64 + ((N) & 15) * 2); PV(R[I], pa_[0], pa_[1]); PV(R[(I) + 1], pa_[2], pa_[3]); \
    asm volatile("" : "+v"(acc[0][0]), "+v"(acc[0][1]), "+v"(acc[0][2]), "+v"(acc[0][3]), "+v"(acc[1][0]), "+v"(acc[1][1]), "+v"(acc[1][2]), "+v"(acc[1][3])); } while (0)
#define DUMP_ACC(BR) do { f32x4 o0, o1; \
    o0[0] = tr32(acc[0][0][0], acc[1][0][0]); o0[1] = tr32(acc[0][0][1], acc[1][0][1]); \
    o0[2] = tr32(acc[0][1][0], acc[1][1][0]); o0[3] = tr32(acc[0][1][1], acc[1][1][1]); \
    o1[0] = tr32(acc[0][2][0], acc[1][2][0]); o1[1] = tr32(acc[0][2][1], acc[1][2][1]); \
    o1[2] = tr32(acc[0][3][0], acc[1][3][0]); o1[3] = tr32(acc[0][3][1], acc[1][3][1]); \
    *(f32x4*)&lds[L_ACC + ((BR) * 8 + wave) * 512 + lane * 8] = o0; \
    *(f32x4*)&lds[L_ACC + ((BR) * 8 + wave) * 512 + lane * 8 + 4] = o1; } while (0)
  PV2(B, 0, 0); SB();
#pragma unroll
  for (int i = 0; i < 16; i += 4) {
    gate(DUE(80 + i / 2));
    A[i / 2] = ldnt(va + SOFF(16 + i / 2) + lane16); A[i / 2 + 1] = ldnt(va + SOFF(17 + i / 2) + lane16);
    SB();
    PV2(B, i + 2, i + 2);
    if (i < 12) PV2(B, i + 4, i + 4);
    SB();
  }
#pragma unroll 1
  for (int it = 2; it < 7; ++it) {
    if (it == 3) EXP_STEP(1);
    if (it == 4) {
      DUMP_ACC(0);
      lds_signal(&lds[F_A], 1u, lane);
#pragma unroll
      for (int c = 0; c < 4; ++c) acc[0][c] = acc[1][c] = f32x2{0.f, 0.f};
    }
    const gptr base = it < 3 ? va : vbb;
#pragma unroll
    for (int i = 0; i < 8; i += 2) {
      const f32x4 pa = *(const f32x4*)(pl + (it >> 1) * 64 + (it & 1) * 16 + i * 2);
      PV(A[i], pa[0], pa[1]); PV(A[i + 1], pa[2], pa[3]);
      const int n = it * 8 + i + 8;
      SB();
      gate(DUE(64 + n));
      A[i] = ldnt(base + SOFF(n) + lane16); A[i + 1] = ldnt(base + SOFF(n + 1) + lane16);
      SB();
    }
  }
  const int soff = WS_P1 + (pr * NWG + kvo * 32 + ((wave >> 1) & 1) * 16 + (lane & 15)) * P1_STRIDE + 4096 + (j >> 3) * 16;
  v4u gs = __builtin_amdgcn_raw_buffer_load_b128(rs, wave < 4 ? 0x7ffffff0 : soff, 0, AUX_SC1);
  SB();
  lds_signal(&lds[F_V], 1u, lane);
  PV2(A, 0, 56); PV2(A, 2, 58); PV2(A, 4, 60); PV2(A, 6, 62);

  DUMP_ACC(1);
  int p1o = WS_P1 + (NWG + e1) * P1_STRIDE + tid * 16;
  int doff = soff - 4096 - (j >> 3) * 16 + (32 * j + (wave & 1) * 16 + (lane >> 4) * 4) * 8;
  int p2o = WS_P2 + wg * P2_STRIDE + tid * 16;
  int roff = WS_P2 + (tid >> 2) * P2_STRIDE + rid * 64 + (tid & 3) * 16;
  int wl = (8 * (lane >> 4) + ((lane & 15) >> 2)) * (DM * 2) + (64 * (wave < 7 ? wave : 0) + 4 * (lane & 3)) * 2, ol = ((lane >> 1) & 1) * 16 + 4 * (lane >> 4);
  asm volatile("" : "+v"(p1o), "+v"(doff), "+v"(p2o), "+v"(roff), "+v"(wl), "+v"(ol));
  int half = (wave - 4) >> 31;
  asm volatile("" : "+s"(half));
  s16x4 wf[8];
#define READ_WO() do { lds_wait(&lds[F_WO], 1u); LAS char* wb = (LAS char*)&lds[L_WO] + wl; \
    _Pragma("unroll") for (int t = 0; t < 8; ++t) wf[t] = __builtin_amdgcn_ds_read_tr16_b64_v4i16((LAS s16x4*)(wb + (t >> 1) * 32 + (t & 1) * 4 * DM * 2)); \
    asm volatile("" : "+v"(wf[0]), "+v"(wf[1]), "+v"(wf[2]), "+v"(wf[3]), "+v"(wf[4]), "+v"(wf[5]), "+v"(wf[6]), "+v"(wf[7])); SB(); } while (0)
  BAR();
  if (half != 0) {
    f32x2 o = *(const f32x2*)&lds[L_ACC + 8 * 512 + 2 * tid];
#pragma unroll
    for (int w = 1; w < 8; ++w) o += *(const f32x2*)&lds[L_ACC + (8 + w) * 512 + 2 * tid];
    __builtin_amdgcn_raw_buffer_store_b128(v4u{f2u(o[0]), tag, f2u(o[1]), tag}, rs, p1o, 0, AUX_SC1);
    READ_WO();
  } else {
    if (__builtin_expect(!__all(gs[1] == tag && gs[3] == tag), 0)) {
      for (unsigned spins = 0; spins < SPIN_CAP; ++spins) {
        asm volatile("" ::: "memory");
        gs = __builtin_amdgcn_raw_buffer_load_b128(rs, soff, 0, AUX_SC1);
        if (__all(gs[1] == tag && gs[3] == tag)) break;
        __builtin_amdgcn_s_sleep(4);
      }
    }
    const float ms = u2f(gs[0]);
    float M = fmaxf(ms, dpp<XOR1>(ms)); M = fmaxf(M, dpp<XOR2>(M)); M = fmaxf(M, dpp<XOR7>(M)); M = fmaxf(M, dpp<ROR8>(M));
    const float f = __builtin_amdgcn_exp2f(ms - M);
    float Lt = f * u2f(gs[2]); Lt += dpp<XOR1>(Lt); Lt += dpp<XOR2>(Lt); Lt += dpp<XOR7>(Lt); Lt += dpp<ROR8>(Lt);
    float wt = f / Lt;
    asm volatile("" : "+v"(wt));
    if (pr) wait_until(DUE(126) + T1ABS);
    v4u ga = __builtin_amdgcn_raw_buffer_load_b128(rs, doff, 0, AUX_SC1), gb = __builtin_amdgcn_raw_buffer_load_b128(rs, doff + 16, 0, AUX_SC1);
    SB();
    READ_WO();
    if (__builtin_expect(!__all(ga[1] == tag && ga[3] == tag && gb[1] == tag && gb[3] == tag), 0)) {
      for (unsigned spins = 0; spins < SPIN_CAP; ++spins) {
        asm volatile("" ::: "memory");
        if (!(ga[1] == tag && ga[3] == tag && gb[1] == tag && gb[3] == tag)) {
          ga = __builtin_amdgcn_raw_buffer_load_b128(rs, doff, 0, AUX_SC1);
          gb = __builtin_amdgcn_raw_buffer_load_b128(rs, doff + 16, 0, AUX_SC1);
        }
        if (__all(ga[1] == tag && ga[3] == tag && gb[1] == tag && gb[3] == tag)) break;
        __builtin_amdgcn_s_sleep(1);
      }
    }
    float o = trd<XOR2>(trd<XOR1>(wt * u2f(ga[0]), wt * u2f(ga[2]), lane & 1), trd<XOR1>(wt * u2f(gb[0]), wt * u2f(gb[2]), lane & 1), lane & 2);
    o += dpp<ROR4>(o); o += dpp<ROR8>(o);
    if (!(lane & 12)) ((unsigned short*)&lds[L_O])[(wave & 3) * 16 + (lane >> 4) * 4 + (lane & 3)] = (unsigned short)f2bf(o);
  }
  BAR();
  if (wave < 7) {
    const bf16x8 af = __builtin_bit_cast(bf16x8, *(const v4u*)&lds[L_O + ol]);
    f32x4 d[4];
#pragma unroll
    for (int t = 0; t < 4; ++t)
      d[t] = __builtin_amdgcn_mfma_f32_16x16x32_bf16(af, __builtin_bit_cast(bf16x8, __builtin_shufflevector(wf[2 * t], wf[2 * t + 1], 0, 1, 2, 3, 4, 5, 6, 7)), f32x4{0.f, 0.f, 0.f, 0.f}, 0, 0, 0);
    const float ya = (lane & 16) ? d[1][0] : d[0][0], yb = (lane & 16) ? d[3][0] : d[2][0], za = (lane & 16) ? d[1][2] : d[0][2], zb = (lane & 16) ? d[3][2] : d[2][2];
    __builtin_amdgcn_raw_buffer_store_b128(v4u{f2u((lane & 32) ? yb : ya), tag, f2u((lane & 32) ? zb : za), tag}, rs, p2o, 0, AUX_SC1);
  }
  if ((wg >> 3) < 14) {
    float gt = lds[L_G + ((lane >> 2) & 1)];
    gt = (lane & 8) ? rbf(1.f - gt) : gt;
    asm volatile("" : "+v"(gt));
    v4u gy[2];
    wait_until(DUE(126) + T2ABS);
    gy[0] = __builtin_amdgcn_raw_buffer_load_b128(rs, roff, 0, AUX_SC1);
    gy[1] = __builtin_amdgcn_raw_buffer_load_b128(rs, roff + 128 * P2_STRIDE, 0, AUX_SC1);
#define GY_OK(i) (gy[i][1] == tag && gy[i][3] == tag)
    if (__builtin_expect(!__all(GY_OK(0) && GY_OK(1)), 0)) {
      for (unsigned spins = 0; spins < SPIN_CAP; ++spins) {
        asm volatile("" ::: "memory");
#pragma unroll
        for (int i = 0; i < 2; ++i) if (!GY_OK(i)) gy[i] = __builtin_amdgcn_raw_buffer_load_b128(rs, roff + i * 128 * P2_STRIDE, 0, AUX_SC1);
        if (__all(GY_OK(0) && GY_OK(1))) break;
        __builtin_amdgcn_s_sleep(1);
      }
    }
    float y = tr16(tr32(u2f(gy[0][0]), u2f(gy[1][0])), tr32(u2f(gy[0][2]), u2f(gy[1][2])));
    y += dpp<ROR8>(y); y += dpp<ROR4>(y);
    if (!(lane & 12)) lds[L_RED + ((lane >> 4) * 4 + (lane & 3)) * 8 + wave] = y;
    BAR();
    if (tid < 16) {
      const f32x4 ta = *(const f32x4*)&lds[L_RED + tid * 8], tc = *(const f32x4*)&lds[L_RED + tid * 8 + 4];
      const float t = rbf(gt * rbf(((ta[0] + ta[1]) + (ta[2] + ta[3])) + ((tc[0] + tc[1]) + (tc[2] + tc[3]))));
      const float rr = t + dpp<ROR8>(t);
      if (tid < 8) outp[(tid >> 2) * DM + rid * 4 + (tid & 3)] = (uint16_t)f2bf(rr);
      if (wg == 0 && tid == 0) __builtin_amdgcn_raw_buffer_store_b32(tag, rs, WS_EPOCH, 0, AUX_SC1);
    }
  }
}

extern "C" void kernel_launch(void* const* d_in, const int* in_sizes, int n_in, void* d_out, int out_size, void* d_ws, size_t ws_size,
                              hipStream_t stream) {
  Params p;
  p.x = (const uint16_t*)d_in[0];
  p.k[0] = (const uint16_t*)d_in[1]; p.v[0] = (const uint16_t*)d_in[2]; p.w[0][0] = (const uint16_t*)d_in[3]; p.w[0][1] = (const uint16_t*)d_in[4];
  p.k[1] = (const uint16_t*)d_in[6]; p.v[1] = (const uint16_t*)d_in[7]; p.w[1][0] = (const uint16_t*)d_in[8]; p.w[1][1] = (const uint16_t*)d_in[9];
  p.gw = (const uint16_t*)d_in[10];
  p.out = (uint16_t*)d_out;
  p.ws = (unsigned char*)d_ws;
  hipLaunchKernelGGL(gqa2_fused, dim3(NWG), dim3(NTHR), 0, stream, p);
}
```
